```python
import torch

def _softmax(x, axis=-1):
    return torch.softmax(x, dim=axis)
def _gelu(x):
    return torch.nn.functional.gelu(x, approximate="tanh")
def _silu(x):
    return torch.nn.functional.silu(x)
def _sigmoid(x):
    return torch.sigmoid(x)
def _layernorm(x, axis=-1, eps=1e-5):
    m = x.mean(dim=axis, keepdim=True)
    v = x.var(dim=axis, keepdim=True, unbiased=False)
    return (x - m) / torch.sqrt(v + eps)
def _scatter_add(tgt, idx, src, axis=0):
    return tgt.clone().index_add_(axis, idx, src)
def _segment_sum(src, seg, n):
    out = torch.zeros((n,) + src.shape[1:], dtype=src.dtype)
    return out.index_add_(0, seg, src)
def _pad(x, pw):
    flat = tuple(v for pair in reversed(pw) for v in pair)
    return torch.nn.functional.pad(x, flat)

kernel_name = 'standalone__deep_cross_xdeepfm__recsys'

def setup_inputs():

    _w = (lambda a: (lambda t: t.to(torch.bfloat16) if t.is_floating_point() else t)(torch.as_tensor(a)))
    B, F, D, L = 1024, 26, 128, 3
    V = min(100000, 100000)
    Din = F * D
    return {
        "idx": _w((lambda lo, hi, *s: torch.randint(lo, hi, s))(0, V, B, F).to(torch.int64)),
        "table": _w((lambda *s: torch.randn(*s, dtype=torch.bfloat16) * 0.8)(V, D)),
        "ws_cin": _w((lambda *s: torch.randn(*s, dtype=torch.bfloat16) * 0.2)(L, F, F, F) * (1.0 / F)),
        "w_cin_out": _w((lambda *s: torch.randn(*s, dtype=torch.bfloat16) * 0.2)(L * F) * ((L * F) ** -0.5)),
        "w_dnn1": _w((lambda *s: torch.randn(*s, dtype=torch.bfloat16) * 0.8)(Din, Din) * (Din ** -0.5)),
        "b_dnn1": _w((lambda *s: torch.randn(*s, dtype=torch.bfloat16) * 3.2)(Din) * 0.1),
        "w_dnn2": _w((lambda *s: torch.randn(*s, dtype=torch.bfloat16) * 0.2)(Din) * (Din ** -0.5)),
        "b_out": _w((lambda *s: torch.randn(*s, dtype=torch.bfloat16) * 0.2)(1)),
    }

def reference(idx, table, ws_cin, w_cin_out, w_dnn1, b_dnn1, w_dnn2, b_out):
    x0 = table[idx]
    B = x0.shape[0]
    L = ws_cin.shape[0]
    xk = x0
    pooled = []
    for l in range(L):
        xk = torch.einsum('bid,bjd,hij->bhd', xk, x0, ws_cin[l])
        pooled.append(xk.sum(axis=-1))
    p_plus = torch.stack(pooled, axis=1).reshape(B, -1)
    logit_cin = p_plus @ w_cin_out
    h = x0.reshape(B, -1)
    h = _gelu(h @ w_dnn1 + b_dnn1)
    logit_dnn = h @ w_dnn2
    return logit_cin + logit_dnn + b_out
```

```cpp
#include <hip/hip_runtime.h>
#include <hip/hip_bf16.h>
typedef unsigned short u16;
__device__ __forceinline__ float bf2f(u16 v){ return __uint_as_float(((unsigned)v)<<16); }
constexpr int B=1024,F=26,D=128,DIN=F*D;
__global__ __launch_bounds__(128) void cin_naive(const int* idx,const u16* table,const u16* W,const u16* wout,float* part){
  int b=blockIdx.x,d=threadIdx.x;
  __shared__ float red[128];
  float x0[F],xk[F],xn[F];
  for(int i=0;i<F;i++){x0[i]=bf2f(table[(size_t)idx[b*F+i]*D+d]);xk[i]=x0[i];}
  float acc=0;
  for(int l=0;l<3;l++){
    #pragma unroll
    for(int h=0;h<F;h++){ float s=0;
      #pragma unroll
      for(int i=0;i<F;i++){ float t=0;
        #pragma unroll
        for(int j=0;j<F;j++) t+=bf2f(W[((l*F+h)*F+i)*F+j])*x0[j];
        s+=t*xk[i]; }
      xn[h]=s; acc+=s*bf2f(wout[l*F+h]); }
    #pragma unroll
    for(int h=0;h<F;h++) xk[h]=xn[h];
  }
  red[d]=acc; __syncthreads();
  if(d==0){float s=0;for(int i=0;i<128;i++)s+=red[i];part[b]=s;}
}
__global__ void dnn_naive(const int* idx,const u16* table,const u16* W1,const u16* b1,const u16* w2,const u16* bo,const float* part,__hip_bfloat16* out){
  int b=blockIdx.x,t=threadIdx.x;
  __shared__ float h[DIN]; __shared__ float red[256];
  for(int k=t;k<DIN;k+=256) h[k]=bf2f(table[(size_t)idx[b*F+k/D]*D+(k%D)]);
  __syncthreads();
  float acc=0;
  for(int n=t;n<DIN;n+=256){ float s=0; for(int k=0;k<DIN;k++) s+=h[k]*bf2f(W1[(size_t)k*DIN+n]);
    s+=bf2f(b1[n]); float u=0.7978845608028654f*(s+0.044715f*s*s*s); float g=0.5f*s*(1.f+tanhf(u)); acc+=g*bf2f(w2[n]); }
  red[t]=acc; __syncthreads();
  if(t==0){float s=0;for(int i=0;i<256;i++)s+=red[i]; out[b]=__float2bfloat16(s+part[b]+bf2f(bo[0]));}
}
extern "C" void kernel_launch(void* const* d_in, const int* in_sizes, int n_in, void* d_out, int out_size, void* d_ws, size_t ws_size, hipStream_t stream) {
  const int* idx=(const int*)d_in[0]; const u16* table=(const u16*)d_in[1]; const u16* W=(const u16*)d_in[2]; const u16* wout=(const u16*)d_in[3];
  const u16* W1=(const u16*)d_in[4]; const u16* b1=(const u16*)d_in[5]; const u16* w2=(const u16*)d_in[6]; const u16* bo=(const u16*)d_in[7];
  cin_naive<<<B,128,0,stream>>>(idx,table,W,wout,(float*)d_ws);
  dnn_naive<<<B,256,0,stream>>>(idx,table,W1,b1,w2,bo,(const float*)d_ws,(__hip_bfloat16*)d_out);
}
```

```cpp
#include <hip/hip_runtime.h>
#include <hip/hip_bf16.h>

typedef unsigned short u16;
typedef unsigned long long u64;
typedef __attribute__((ext_vector_type(2))) float f32x2;
typedef __attribute__((ext_vector_type(2))) unsigned u32x2;
typedef __attribute__((ext_vector_type(4))) unsigned u32x4;
typedef __attribute__((ext_vector_type(4))) short s16x4;
typedef __attribute__((ext_vector_type(2))) short s16x2;
typedef __attribute__((ext_vector_type(2))) __bf16 bf16x2;
typedef __attribute__((ext_vector_type(8))) __bf16 bf16x8;
typedef __attribute__((ext_vector_type(2))) _Float16 f16x2;
typedef __attribute__((ext_vector_type(8))) _Float16 f16x8;
typedef __attribute__((ext_vector_type(16))) float f32x16;
typedef __attribute__((ext_vector_type(4))) float f32x4;
typedef __attribute__((ext_vector_type(16))) unsigned u32x16;
typedef __attribute__((ext_vector_type(8))) unsigned u32x8;
#define LDS_AS __attribute__((address_space(3)))
#define GLB_AS __attribute__((address_space(1)))
#define CST_AS __attribute__((address_space(4)))

#define CIN_UNIT_ASM \
  "ds_read_b32 v65, %[xlo] offset:0\n" \
  "ds_read_b32 v67, %[xlo] offset:128\n" \
  "ds_read_b32 v69, %[xlo] offset:256\n" \
  "ds_read_b32 v71, %[xlo] offset:384\n" \
  "ds_read_b32 v73, %[xlo] offset:512\n" \
  "ds_read_b32 v75, %[xlo] offset:640\n" \
  "ds_read_b32 v77, %[xlo] offset:768\n" \
  "ds_read_b32 v79, %[xlo] offset:896\n" \
  "ds_read_b32 v81, %[xlo] offset:1024\n" \
  "ds_read_b32 v83, %[xlo] offset:1152\n" \
  "ds_read_b32 v85, %[xlo] offset:1280\n" \
  "ds_read_b32 v87, %[xlo] offset:1408\n" \
  "ds_read_b32 v89, %[xlo] offset:1536\n" \
  "s_cmp_eq_u32 %[off], 0\n" \
  "s_cbranch_scc1 7f\n" \
  "v_lshlrev_b32 v222, 0, %[wl]\n" \
  "s_waitcnt vmcnt(0)\n" \
  "s_barrier\n" \
  "s_add_u32 m0, %[wm], 0\n" \
  "s_add_u32 vcc_lo, %[wo], 0\n" \
  "buffer_load_dwordx4 v222, %[rsf], vcc_lo offen sc1 lds\n" \
  "s_add_u32 m0, %[wm], 4224\n" \
  "s_add_u32 vcc_lo, %[wo], 4096\n" \
  "buffer_load_dwordx4 v222, %[rsf], vcc_lo offen sc1 lds\n" \
  "s_add_u32 m0, %[wm], 8448\n" \
  "s_add_u32 vcc_lo, %[wo], 8192\n" \
  "buffer_load_dwordx4 v222, %[rsf], vcc_lo offen sc1 lds\n" \
  "s_add_u32 m0, %[wm], 12672\n" \
  "s_add_u32 vcc_lo, %[wo], 12288\n" \
  "buffer_load_dwordx4 v222, %[rsf], vcc_lo offen sc1 lds\n" \
  "s_add_u32 m0, %[wm], 16896\n" \
  "s_add_u32 vcc_lo, %[wo], 16384\n" \
  "buffer_load_dwordx4 v222, %[rsf], vcc_lo offen sc1 lds\n" \
  "s_add_u32 m0, %[wm], 21120\n" \
  "s_add_u32 vcc_lo, %[wo], 20480\n" \
  "buffer_load_dwordx4 v222, %[rsf], vcc_lo offen sc1 lds\n" \
  "s_add_u32 m0, %[wm], 25344\n" \
  "s_add_u32 vcc_lo, %[wo], 24576\n" \
  "buffer_load_dwordx4 v222, %[rsf], vcc_lo offen sc1 lds\n" \
  "s_add_u32 m0, %[wm], 29568\n" \
  "s_add_u32 vcc_lo, %[wo], 28672\n" \
  "buffer_load_dwordx4 v222, %[rsf], vcc_lo offen sc1 lds\n" \
  "s_add_u32 m0, %[wm], 33792\n" \
  "s_add_u32 vcc_lo, %[wo], 32768\n" \
  "buffer_load_dwordx4 v222, %[rsf], vcc_lo offen sc1 lds\n" \
  "s_add_u32 m0, %[wm], 38016\n" \
  "s_add_u32 vcc_lo, %[wo], 36864\n" \
  "buffer_load_dwordx4 v222, %[rsf], vcc_lo offen sc1 lds\n" \
  "s_add_u32 m0, %[wm], 42240\n" \
  "s_add_u32 vcc_lo, %[wo], 40960\n" \
  "buffer_load_dwordx4 v222, %[rsf], vcc_lo offen sc1 lds\n" \
  "s_add_u32 m0, %[wm], 46464\n" \
  "s_add_u32 vcc_lo, %[wo], 45056\n" \
  "buffer_load_dwordx4 v222, %[rsf], vcc_lo offen sc1 lds\n" \
  "7:\n" \
  "s_waitcnt lgkmcnt(9)\n" \
  "ds_read_b32 v91, %[xhi] offset:1664\n" \
  "ds_read_b32 v93, %[xhi] offset:1792\n" \
  "ds_read_b32 v95, %[xhi] offset:1920\n" \
  "ds_read_b32 v97, %[xhi] offset:2048\n" \
  "ds_read_b32 v99, %[xhi] offset:2176\n" \
  "ds_read_b32 v101, %[xhi] offset:2304\n" \
  "v_perm_b32 v186, v67, v65, %[psel]\n" \
  "v_perm_b32 v190, v67, v65, %[pselh]\n" \
  "v_perm_b32 v187, v71, v69, %[psel]\n" \
  "v_perm_b32 v191, v71, v69, %[pselh]\n" \
  "s_waitcnt lgkmcnt(11)\n" \
  "ds_read_b32 v103, %[xhi] offset:2432\n" \
  "ds_read_b32 v105, %[xhi] offset:2560\n" \
  "ds_read_b32 v107, %[xhi] offset:2688\n" \
  "ds_read_b32 v109, %[xhi] offset:2816\n" \
  "v_perm_b32 v188, v75, v73, %[psel]\n" \
  "v_perm_b32 v192, v75, v73, %[pselh]\n" \
  "v_perm_b32 v189, v79, v77, %[psel]\n" \
  "v_perm_b32 v193, v79, v77, %[pselh]\n" \
  "s_waitcnt lgkmcnt(11)\n" \
  "ds_read_b32 v111, %[xhi] offset:2944\n" \
  "ds_read_b32 v113, %[xhi] offset:3072\n" \
  "ds_read_b32 v115, %[xhi] offset:3200\n" \
  "v_perm_b32 v194, v83, v81, %[psel]\n" \
  "v_perm_b32 v198, v83, v81, %[pselh]\n" \
  "v_perm_b32 v195, v87, v85, %[psel]\n" \
  "v_perm_b32 v199, v87, v85, %[pselh]\n" \
  "s_waitcnt lgkmcnt(13)\n" \
  "v_and_b32 v196, 0xffff, v89\n" \
  "v_lshrrev_b32 v200, 16, v89\n" \
  "v_mov_b32 v197, 0\n" \
  "v_mov_b32 v201, 0\n" \
  "v_lshlrev_b32 v64, 16, v65\n" \
  "v_max_f32 v218, |v64|, |v65|\n" \
  "v_lshlrev_b32 v66, 16, v67\n" \
  "v_max_f32 v219, |v66|, |v67|\n" \
  "v_lshlrev_b32 v68, 16, v69\n" \
  "v_max3_f32 v218, |v68|, |v69|, v218\n" \
  "v_lshlrev_b32 v70, 16, v71\n" \
  "v_max3_f32 v219, |v70|, |v71|, v219\n" \
  "v_lshlrev_b32 v72, 16, v73\n" \
  "v_max3_f32 v218, |v72|, |v73|, v218\n" \
  "v_lshlrev_b32 v74, 16, v75\n" \
  "v_max3_f32 v219, |v74|, |v75|, v219\n" \
  "v_lshlrev_b32 v76, 16, v77\n" \
  "v_max3_f32 v218, |v76|, |v77|, v218\n" \
  "v_lshlrev_b32 v78, 16, v79\n" \
  "v_max3_f32 v219, |v78|, |v79|, v219\n" \
  "v_lshlrev_b32 v80, 16, v81\n" \
  "v_max3_f32 v218, |v80|, |v81|, v218\n" \
  "v_lshlrev_b32 v82, 16, v83\n" \
  "v_max3_f32 v219, |v82|, |v83|, v219\n" \
  "v_lshlrev_b32 v84, 16, v85\n" \
  "v_max3_f32 v218, |v84|, |v85|, v218\n" \
  "v_lshlrev_b32 v86, 16, v87\n" \
  "v_max3_f32 v219, |v86|, |v87|, v219\n" \
  "v_lshlrev_b32 v88, 16, v89\n" \
  "v_max3_f32 v218, |v88|, |v89|, v218\n" \
  "s_waitcnt lgkmcnt(9)\n" \
  "v_lshlrev_b32 v90, 16, v91\n" \
  "v_max3_f32 v219, |v90|, |v91|, v219\n" \
  "v_lshlrev_b32 v92, 16, v93\n" \
  "v_max3_f32 v218, |v92|, |v93|, v218\n" \
  "v_lshlrev_b32 v94, 16, v95\n" \
  "v_max3_f32 v219, |v94|, |v95|, v219\n" \
  "v_lshlrev_b32 v96, 16, v97\n" \
  "v_max3_f32 v218, |v96|, |v97|, v218\n" \
  "s_waitcnt lgkmcnt(5)\n" \
  "v_lshlrev_b32 v98, 16, v99\n" \
  "v_max3_f32 v219, |v98|, |v99|, v219\n" \
  "v_lshlrev_b32 v100, 16, v101\n" \
  "v_max3_f32 v218, |v100|, |v101|, v218\n" \
  "v_lshlrev_b32 v102, 16, v103\n" \
  "v_max3_f32 v219, |v102|, |v103|, v219\n" \
  "v_lshlrev_b32 v104, 16, v105\n" \
  "v_max3_f32 v218, |v104|, |v105|, v218\n" \
  "s_waitcnt lgkmcnt(1)\n" \
  "v_lshlrev_b32 v106, 16, v107\n" \
  "v_max3_f32 v219, |v106|, |v107|, v219\n" \
  "v_lshlrev_b32 v108, 16, v109\n" \
  "v_max3_f32 v218, |v108|, |v109|, v218\n" \
  "v_lshlrev_b32 v110, 16, v111\n" \
  "v_max3_f32 v219, |v110|, |v111|, v219\n" \
  "v_lshlrev_b32 v112, 16, v113\n" \
  "v_max3_f32 v218, |v112|, |v113|, v218\n" \
  "s_waitcnt lgkmcnt(0)\n" \
  "v_lshlrev_b32 v114, 16, v115\n" \
  "v_max3_f32 v219, |v114|, |v115|, v219\n" \
  "v_max_f32 v218, v218, v219\n" \
  "v_frexp_exp_i32_f32 v221, v218\n" \
  "v_max_i32 v221, 0xffffff89, v221\n" \
  "v_sub_u32 v220, 0x87, v221\n" \
  "v_lshlrev_b32 v220, 23, v220\n" \
  "v_mul_f32 v64, v64, v220\n" \
  "v_mul_f32 v90, v90, v220\n" \
  "v_mul_f32 v65, v65, v220\n" \
  "v_mul_f32 v91, v91, v220\n" \
  "v_cvt_pkrtz_f16_f32 v160, v64, v90\n" \
  "v_cvt_pkrtz_f16_f32 v161, v65, v91\n" \
  "v_and_b32 v161, 0xfff8fff8, v161\n" \
  "v_mul_f32 v66, v66, v220\n" \
  "v_mul_f32 v92, v92, v220\n" \
  "v_mul_f32 v67, v67, v220\n" \
  "v_mul_f32 v93, v93, v220\n" \
  "v_cvt_pkrtz_f16_f32 v162, v66, v92\n" \
  "v_cvt_pkrtz_f16_f32 v163, v67, v93\n" \
  "v_and_b32 v163, 0xfff8fff8, v163\n" \
  "v_mul_f32 v68, v68, v220\n" \
  "v_mul_f32 v94, v94, v220\n" \
  "v_mul_f32 v69, v69, v220\n" \
  "v_mul_f32 v95, v95, v220\n" \
  "v_cvt_pkrtz_f16_f32 v164, v68, v94\n" \
  "v_cvt_pkrtz_f16_f32 v165, v69, v95\n" \
  "v_and_b32 v165, 0xfff8fff8, v165\n" \
  "v_mul_f32 v70, v70, v220\n" \
  "v_mul_f32 v96, v96, v220\n" \
  "v_mul_f32 v71, v71, v220\n" \
  "v_mul_f32 v97, v97, v220\n" \
  "v_cvt_pkrtz_f16_f32 v166, v70, v96\n" \
  "v_cvt_pkrtz_f16_f32 v167, v71, v97\n" \
  "v_and_b32 v167, 0xfff8fff8, v167\n" \
  "v_mul_f32 v72, v72, v220\n" \
  "v_mul_f32 v98, v98, v220\n" \
  "v_mul_f32 v73, v73, v220\n" \
  "v_mul_f32 v99, v99, v220\n" \
  "v_cvt_pkrtz_f16_f32 v168, v72, v98\n" \
  "v_cvt_pkrtz_f16_f32 v169, v73, v99\n" \
  "v_and_b32 v169, 0xfff8fff8, v169\n" \
  "v_mul_f32 v74, v74, v220\n" \
  "v_mul_f32 v100, v100, v220\n" \
  "v_mul_f32 v75, v75, v220\n" \
  "v_mul_f32 v101, v101, v220\n" \
  "v_cvt_pkrtz_f16_f32 v170, v74, v100\n" \
  "v_cvt_pkrtz_f16_f32 v171, v75, v101\n" \
  "v_and_b32 v171, 0xfff8fff8, v171\n" \
  "v_mul_f32 v76, v76, v220\n" \
  "v_mul_f32 v102, v102, v220\n" \
  "v_mul_f32 v77, v77, v220\n" \
  "v_mul_f32 v103, v103, v220\n" \
  "v_cvt_pkrtz_f16_f32 v172, v76, v102\n" \
  "v_cvt_pkrtz_f16_f32 v173, v77, v103\n" \
  "v_and_b32 v173, 0xfff8fff8, v173\n" \
  "v_mul_f32 v78, v78, v220\n" \
  "v_mul_f32 v104, v104, v220\n" \
  "v_mul_f32 v79, v79, v220\n" \
  "v_mul_f32 v105, v105, v220\n" \
  "v_cvt_pkrtz_f16_f32 v174, v78, v104\n" \
  "v_cvt_pkrtz_f16_f32 v175, v79, v105\n" \
  "v_and_b32 v175, 0xfff8fff8, v175\n" \
  "v_mul_f32 v80, v80, v220\n" \
  "v_mul_f32 v106, v106, v220\n" \
  "v_mul_f32 v81, v81, v220\n" \
  "v_mul_f32 v107, v107, v220\n" \
  "v_cvt_pkrtz_f16_f32 v176, v80, v106\n" \
  "v_cvt_pkrtz_f16_f32 v177, v81, v107\n" \
  "v_and_b32 v177, 0xfff8fff8, v177\n" \
  "v_mul_f32 v82, v82, v220\n" \
  "v_mul_f32 v108, v108, v220\n" \
  "v_mul_f32 v83, v83, v220\n" \
  "v_mul_f32 v109, v109, v220\n" \
  "v_cvt_pkrtz_f16_f32 v178, v82, v108\n" \
  "v_cvt_pkrtz_f16_f32 v179, v83, v109\n" \
  "v_and_b32 v179, 0xfff8fff8, v179\n" \
  "v_mul_f32 v84, v84, v220\n" \
  "v_mul_f32 v110, v110, v220\n" \
  "v_mul_f32 v85, v85, v220\n" \
  "v_mul_f32 v111, v111, v220\n" \
  "v_cvt_pkrtz_f16_f32 v180, v84, v110\n" \
  "v_cvt_pkrtz_f16_f32 v181, v85, v111\n" \
  "v_and_b32 v181, 0xfff8fff8, v181\n" \
  "s_cmp_eq_u32 %[off], 0\n" \
  "s_cbranch_scc1 9f\n" \
  "v_mov_b32 v222, %[off]\n" \
  "v_mov_b32 v223, 1\n" \
  "s_waitcnt vmcnt(0)\n" \
  "s_mov_b64 exec, 1\n" \
  "ds_add_u32 v222, v223\n" \
  "s_mov_b64 exec, -1\n" \
  "8:\n" \
  "ds_read_b32 v223, v222\n" \
  "s_waitcnt lgkmcnt(0)\n" \
  "v_cmp_gt_u32 vcc, 4, v223\n" \
  "s_cbranch_vccnz 8b\n" \
  "9:\n" \
  "ds_read_b128 v[206:209], %[wl] offset:46464\n" \
  "ds_read_b128 v[214:217], %[wl] offset:47520\n" \
  "v_mul_f32 v86, v86, v220\n" \
  "v_mul_f32 v112, v112, v220\n" \
  "v_mul_f32 v87, v87, v220\n" \
  "v_mul_f32 v113, v113, v220\n" \
  "v_cvt_pkrtz_f16_f32 v182, v86, v112\n" \
  "v_cvt_pkrtz_f16_f32 v183, v87, v113\n" \
  "v_and_b32 v183, 0xfff8fff8, v183\n" \
  "v_mul_f32 v88, v88, v220\n" \
  "v_mul_f32 v114, v114, v220\n" \
  "v_mul_f32 v89, v89, v220\n" \
  "v_mul_f32 v115, v115, v220\n" \
  "v_cvt_pkrtz_f16_f32 v184, v88, v114\n" \
  "v_cvt_pkrtz_f16_f32 v185, v89, v115\n" \
  "v_and_b32 v185, 0xfff8fff8, v185\n" \
  "s_waitcnt lgkmcnt(1)\n" \
  "s_setprio 1\n" \
  "v_mfma_f32_32x32x16_bf16 v[128:143], v[206:209], v[186:189], %[a1]\n" \
  "v_mfma_f32_32x32x16_bf16 v[144:159], v[206:209], v[190:193], %[a1]\n" \
  "ds_read_b128 v[202:205], %[wl] offset:0\n" \
  "ds_read_b128 v[206:209], %[wl] offset:23232\n" \
  "v_pk_mul_f16 v186, v160, v162 op_sel:[0,0] op_sel_hi:[0,1]\n" \
  "v_pk_mul_f16 v190, v161, v163 op_sel:[0,0] op_sel_hi:[0,1]\n" \
  "v_pk_mul_f16 v187, v160, v164 op_sel:[0,0] op_sel_hi:[0,1]\n" \
  "v_pk_mul_f16 v191, v161, v165 op_sel:[0,0] op_sel_hi:[0,1]\n" \
  "s_waitcnt lgkmcnt(2)\n" \
  "v_mfma_f32_32x32x16_bf16 v[128:143], v[214:217], v[194:197], v[128:143]\n" \
  "v_pk_mul_f16 v188, v162, v164 op_sel:[0,0] op_sel_hi:[0,1]\n" \
  "v_pk_mul_f16 v192, v163, v165 op_sel:[0,0] op_sel_hi:[0,1]\n" \
  "v_pk_mul_f16 v189, v160, v166 op_sel:[0,0] op_sel_hi:[0,1]\n" \
  "v_pk_mul_f16 v193, v161, v167 op_sel:[0,0] op_sel_hi:[0,1]\n" \
  "v_mfma_f32_32x32x16_bf16 v[144:159], v[214:217], v[198:201], v[144:159]\n" \
  "ds_read_b128 v[210:213], %[wl] offset:1056\n" \
  "ds_read_b128 v[214:217], %[wl] offset:24288\n" \
  "v_pk_mul_f16 v194, v162, v166 op_sel:[0,0] op_sel_hi:[0,1]\n" \
  "v_pk_mul_f16 v198, v163, v167 op_sel:[0,0] op_sel_hi:[0,1]\n" \
  "s_waitcnt lgkmcnt(3)\n" \
  "v_mfma_f32_32x32x16_f16 v[64:79], v[202:205], v[186:189], 0\n" \
  "v_pk_mul_f16 v195, v164, v166 op_sel:[0,0] op_sel_hi:[0,1]\n" \
  "v_pk_mul_f16 v199, v165, v167 op_sel:[0,0] op_sel_hi:[0,1]\n" \
  "s_waitcnt lgkmcnt(2)\n" \
  "v_mfma_f32_32x32x16_f16 v[80:95], v[206:209], v[186:189], 0\n" \
  "v_pk_mul_f16 v196, v160, v168 op_sel:[0,0] op_sel_hi:[0,1]\n" \
  "v_pk_mul_f16 v200, v161, v169 op_sel:[0,0] op_sel_hi:[0,1]\n" \
  "v_mfma_f32_32x32x16_f16 v[96:111], v[202:205], v[190:193], 0\n" \
  "v_pk_mul_f16 v197, v162, v168 op_sel:[0,0] op_sel_hi:[0,1]\n" \
  "v_pk_mul_f16 v201, v163, v169 op_sel:[0,0] op_sel_hi:[0,1]\n" \
  "v_mfma_f32_32x32x16_f16 v[112:127], v[206:209], v[190:193], 0\n" \
  "ds_read_b128 v[202:205], %[wl] offset:2112\n" \
  "ds_read_b128 v[206:209], %[wl] offset:25344\n" \
  "v_pk_mul_f16 v186, v164, v168 op_sel:[0,0] op_sel_hi:[0,1]\n" \
  "v_pk_mul_f16 v190, v165, v169 op_sel:[0,0] op_sel_hi:[0,1]\n" \
  "s_waitcnt lgkmcnt(3)\n" \
  "s_setprio 2\n" \
  "v_mfma_f32_32x32x16_f16 v[64:79], v[210:213], v[194:197], v[64:79]\n" \
  "v_pk_mul_f16 v187, v166, v168 op_sel:[0,0] op_sel_hi:[0,1]\n" \
  "v_pk_mul_f16 v191, v167, v169 op_sel:[0,0] op_sel_hi:[0,1]\n" \
  "s_waitcnt lgkmcnt(2)\n" \
  "v_mfma_f32_32x32x16_f16 v[80:95], v[214:217], v[194:197], v[80:95]\n" \
  "v_pk_mul_f16 v188, v160, v170 op_sel:[0,0] op_sel_hi:[0,1]\n" \
  "v_pk_mul_f16 v192, v161, v171 op_sel:[0,0] op_sel_hi:[0,1]\n" \
  "v_mfma_f32_32x32x16_f16 v[96:111], v[210:213], v[198:201], v[96:111]\n" \
  "v_pk_mul_f16 v189, v162, v170 op_sel:[0,0] op_sel_hi:[0,1]\n" \
  "v_pk_mul_f16 v193, v163, v171 op_sel:[0,0] op_sel_hi:[0,1]\n" \
  "v_mfma_f32_32x32x16_f16 v[112:127], v[214:217], v[198:201], v[112:127]\n" \
  "ds_read_b128 v[210:213], %[wl] offset:3168\n" \
  "ds_read_b128 v[214:217], %[wl] offset:26400\n" \
  "v_pk_mul_f16 v194, v164, v170 op_sel:[0,0] op_sel_hi:[0,1]\n" \
  "v_pk_mul_f16 v198, v165, v171 op_sel:[0,0] op_sel_hi:[0,1]\n" \
  "s_waitcnt lgkmcnt(3)\n" \
  "v_mfma_f32_32x32x16_f16 v[64:79], v[202:205], v[186:189], v[64:79]\n" \
  "v_pk_mul_f16 v195, v166, v170 op_sel:[0,0] op_sel_hi:[0,1]\n" \
  "v_pk_mul_f16 v199, v167, v171 op_sel:[0,0] op_sel_hi:[0,1]\n" \
  "s_waitcnt lgkmcnt(2)\n" \
  "v_mfma_f32_32x32x16_f16 v[80:95], v[206:209], v[186:189], v[80:95]\n" \
  "v_pk_mul_f16 v196, v168, v170 op_sel:[0,0] op_sel_hi:[0,1]\n" \
  "v_pk_mul_f16 v200, v169, v171 op_sel:[0,0] op_sel_hi:[0,1]\n" \
  "v_mfma_f32_32x32x16_f16 v[96:111], v[202:205], v[190:193], v[96:111]\n" \
  "v_pk_mul_f16 v197, v160, v172 op_sel:[0,0] op_sel_hi:[0,1]\n" \
  "v_pk_mul_f16 v201, v161, v173 op_sel:[0,0] op_sel_hi:[0,1]\n" \
  "v_mfma_f32_32x32x16_f16 v[112:127], v[206:209], v[190:193], v[112:127]\n" \
  "ds_read_b128 v[202:205], %[wl] offset:4224\n" \
  "ds_read_b128 v[206:209], %[wl] offset:27456\n" \
  "v_pk_mul_f16 v186, v162, v172 op_sel:[0,0] op_sel_hi:[0,1]\n" \
  "v_pk_mul_f16 v190, v163, v173 op_sel:[0,0] op_sel_hi:[0,1]\n" \
  "s_waitcnt lgkmcnt(3)\n" \
  "v_mfma_f32_32x32x16_f16 v[64:79], v[210:213], v[194:197], v[64:79]\n" \
  "v_pk_mul_f16 v187, v164, v172 op_sel:[0,0] op_sel_hi:[0,1]\n" \
  "v_pk_mul_f16 v191, v165, v173 op_sel:[0,0] op_sel_hi:[0,1]\n" \
  "s_waitcnt lgkmcnt(2)\n" \
  "v_mfma_f32_32x32x16_f16 v[80:95], v[214:217], v[194:197], v[80:95]\n" \
  "v_pk_mul_f16 v188, v166, v172 op_sel:[0,0] op_sel_hi:[0,1]\n" \
  "v_pk_mul_f16 v192, v167, v173 op_sel:[0,0] op_sel_hi:[0,1]\n" \
  "v_mfma_f32_32x32x16_f16 v[96:111], v[210:213], v[198:201], v[96:111]\n" \
  "v_pk_mul_f16 v189, v168, v172 op_sel:[0,0] op_sel_hi:[0,1]\n" \
  "v_pk_mul_f16 v193, v169, v173 op_sel:[0,0] op_sel_hi:[0,1]\n" \
  "v_mfma_f32_32x32x16_f16 v[112:127], v[214:217], v[198:201], v[112:127]\n" \
  "ds_read_b128 v[210:213], %[wl] offset:5280\n" \
  "ds_read_b128 v[214:217], %[wl] offset:28512\n" \
  "v_pk_mul_f16 v194, v170, v172 op_sel:[0,0] op_sel_hi:[0,1]\n" \
  "v_pk_mul_f16 v198, v171, v173 op_sel:[0,0] op_sel_hi:[0,1]\n" \
  "s_waitcnt lgkmcnt(3)\n" \
  "v_mfma_f32_32x32x16_f16 v[64:79], v[202:205], v[186:189], v[64:79]\n" \
  "v_pk_mul_f16 v195, v160, v174 op_sel:[0,0] op_sel_hi:[0,1]\n" \
  "v_pk_mul_f16 v199, v161, v175 op_sel:[0,0] op_sel_hi:[0,1]\n" \
  "s_waitcnt lgkmcnt(2)\n" \
  "v_mfma_f32_32x32x16_f16 v[80:95], v[206:209], v[186:189], v[80:95]\n" \
  "v_pk_mul_f16 v196, v162, v174 op_sel:[0,0] op_sel_hi:[0,1]\n" \
  "v_pk_mul_f16 v200, v163, v175 op_sel:[0,0] op_sel_hi:[0,1]\n" \
  "v_mfma_f32_32x32x16_f16 v[96:111], v[202:205], v[190:193], v[96:111]\n" \
  "v_pk_mul_f16 v197, v164, v174 op_sel:[0,0] op_sel_hi:[0,1]\n" \
  "v_pk_mul_f16 v201, v165, v175 op_sel:[0,0] op_sel_hi:[0,1]\n" \
  "v_mfma_f32_32x32x16_f16 v[112:127], v[206:209], v[190:193], v[112:127]\n" \
  "ds_read_b128 v[202:205], %[wl] offset:6336\n" \
  "ds_read_b128 v[206:209], %[wl] offset:29568\n" \
  "v_pk_mul_f16 v186, v166, v174 op_sel:[0,0] op_sel_hi:[0,1]\n" \
  "v_pk_mul_f16 v190, v167, v175 op_sel:[0,0] op_sel_hi:[0,1]\n" \
  "s_waitcnt lgkmcnt(3)\n" \
  "s_setprio 3\n" \
  "v_mfma_f32_32x32x16_f16 v[64:79], v[210:213], v[194:197], v[64:79]\n" \
  "v_pk_mul_f16 v187, v168, v174 op_sel:[0,0] op_sel_hi:[0,1]\n" \
  "v_pk_mul_f16 v191, v169, v175 op_sel:[0,0] op_sel_hi:[0,1]\n" \
  "s_waitcnt lgkmcnt(2)\n" \
  "v_mfma_f32_32x32x16_f16 v[80:95], v[214:217], v[194:197], v[80:95]\n" \
  "v_pk_mul_f16 v188, v170, v174 op_sel:[0,0] op_sel_hi:[0,1]\n" \
  "v_pk_mul_f16 v192, v171, v175 op_sel:[0,0] op_sel_hi:[0,1]\n" \
  "v_mfma_f32_32x32x16_f16 v[96:111], v[210:213], v[198:201], v[96:111]\n" \
  "v_pk_mul_f16 v189, v172, v174 op_sel:[0,0] op_sel_hi:[0,1]\n" \
  "v_pk_mul_f16 v193, v173, v175 op_sel:[0,0] op_sel_hi:[0,1]\n" \
  "v_mfma_f32_32x32x16_f16 v[112:127], v[214:217], v[198:201], v[112:127]\n" \
  "ds_read_b128 v[210:213], %[wl] offset:7392\n" \
  "ds_read_b128 v[214:217], %[wl] offset:30624\n" \
  "v_pk_mul_f16 v194, v160, v176 op_sel:[0,0] op_sel_hi:[0,1]\n" \
  "v_pk_mul_f16 v198, v161, v177 op_sel:[0,0] op_sel_hi:[0,1]\n" \
  "s_waitcnt lgkmcnt(3)\n" \
  "v_mfma_f32_32x32x16_f16 v[64:79], v[202:205], v[186:189], v[64:79]\n" \
  "v_pk_mul_f16 v195, v162, v176 op_sel:[0,0] op_sel_hi:[0,1]\n" \
  "v_pk_mul_f16 v199, v163, v177 op_sel:[0,0] op_sel_hi:[0,1]\n" \
  "s_waitcnt lgkmcnt(2)\n" \
  "v_mfma_f32_32x32x16_f16 v[80:95], v[206:209], v[186:189], v[80:95]\n" \
  "v_pk_mul_f16 v196, v164, v176 op_sel:[0,0] op_sel_hi:[0,1]\n" \
  "v_pk_mul_f16 v200, v165, v177 op_sel:[0,0] op_sel_hi:[0,1]\n" \
  "v_mfma_f32_32x32x16_f16 v[96:111], v[202:205], v[190:193], v[96:111]\n" \
  "v_pk_mul_f16 v197, v166, v176 op_sel:[0,0] op_sel_hi:[0,1]\n" \
  "v_pk_mul_f16 v201, v167, v177 op_sel:[0,0] op_sel_hi:[0,1]\n" \
  "v_mfma_f32_32x32x16_f16 v[112:127], v[206:209], v[190:193], v[112:127]\n" \
  "ds_read_b128 v[202:205], %[wl] offset:8448\n" \
  "ds_read_b128 v[206:209], %[wl] offset:31680\n" \
  "v_pk_mul_f16 v186, v168, v176 op_sel:[0,0] op_sel_hi:[0,1]\n" \
  "v_pk_mul_f16 v190, v169, v177 op_sel:[0,0] op_sel_hi:[0,1]\n" \
  "s_waitcnt lgkmcnt(3)\n" \
  "v_mfma_f32_32x32x16_f16 v[64:79], v[210:213], v[194:197], v[64:79]\n" \
  "v_pk_mul_f16 v187, v170, v176 op_sel:[0,0] op_sel_hi:[0,1]\n" \
  "v_pk_mul_f16 v191, v171, v177 op_sel:[0,0] op_sel_hi:[0,1]\n" \
  "s_waitcnt lgkmcnt(2)\n" \
  "v_mfma_f32_32x32x16_f16 v[80:95], v[214:217], v[194:197], v[80:95]\n" \
  "v_pk_mul_f16 v188, v172, v176 op_sel:[0,0] op_sel_hi:[0,1]\n" \
  "v_pk_mul_f16 v192, v173, v177 op_sel:[0,0] op_sel_hi:[0,1]\n" \
  "v_mfma_f32_32x32x16_f16 v[96:111], v[210:213], v[198:201], v[96:111]\n" \
  "v_pk_mul_f16 v189, v174, v176 op_sel:[0,0] op_sel_hi:[0,1]\n" \
  "v_pk_mul_f16 v193, v175, v177 op_sel:[0,0] op_sel_hi:[0,1]\n" \
  "v_mfma_f32_32x32x16_f16 v[112:127], v[214:217], v[198:201], v[112:127]\n" \
  "ds_read_b128 v[210:213], %[wl] offset:9504\n" \
  "ds_read_b128 v[214:217], %[wl] offset:32736\n" \
  "v_pk_mul_f16 v194, v160, v178 op_sel:[0,0] op_sel_hi:[0,1]\n" \
  "v_pk_mul_f16 v198, v161, v179 op_sel:[0,0] op_sel_hi:[0,1]\n" \
  "s_waitcnt lgkmcnt(3)\n" \
  "v_mfma_f32_32x32x16_f16 v[64:79], v[202:205], v[186:189], v[64:79]\n" \
  "v_pk_mul_f16 v195, v162, v178 op_sel:[0,0] op_sel_hi:[0,1]\n" \
  "v_pk_mul_f16 v199, v163, v179 op_sel:[0,0] op_sel_hi:[0,1]\n" \
  "s_waitcnt lgkmcnt(2)\n" \
  "v_mfma_f32_32x32x16_f16 v[80:95], v[206:209], v[186:189], v[80:95]\n" \
  "v_pk_mul_f16 v196, v164, v178 op_sel:[0,0] op_sel_hi:[0,1]\n" \
  "v_pk_mul_f16 v200, v165, v179 op_sel:[0,0] op_sel_hi:[0,1]\n" \
  "v_mfma_f32_32x32x16_f16 v[96:111], v[202:205], v[190:193], v[96:111]\n" \
  "v_pk_mul_f16 v197, v166, v178 op_sel:[0,0] op_sel_hi:[0,1]\n" \
  "v_pk_mul_f16 v201, v167, v179 op_sel:[0,0] op_sel_hi:[0,1]\n" \
  "v_mfma_f32_32x32x16_f16 v[112:127], v[206:209], v[190:193], v[112:127]\n" \
  "ds_read_b128 v[202:205], %[wl] offset:10560\n" \
  "ds_read_b128 v[206:209], %[wl] offset:33792\n" \
  "v_pk_mul_f16 v186, v168, v178 op_sel:[0,0] op_sel_hi:[0,1]\n" \
  "v_pk_mul_f16 v190, v169, v179 op_sel:[0,0] op_sel_hi:[0,1]\n" \
  "s_waitcnt lgkmcnt(3)\n" \
  "v_mfma_f32_32x32x16_f16 v[64:79], v[210:213], v[194:197], v[64:79]\n" \
  "v_pk_mul_f16 v187, v170, v178 op_sel:[0,0] op_sel_hi:[0,1]\n" \
  "v_pk_mul_f16 v191, v171, v179 op_sel:[0,0] op_sel_hi:[0,1]\n" \
  "s_waitcnt lgkmcnt(2)\n" \
  "v_mfma_f32_32x32x16_f16 v[80:95], v[214:217], v[194:197], v[80:95]\n" \
  "v_pk_mul_f16 v188, v172, v178 op_sel:[0,0] op_sel_hi:[0,1]\n" \
  "v_pk_mul_f16 v192, v173, v179 op_sel:[0,0] op_sel_hi:[0,1]\n" \
  "v_mfma_f32_32x32x16_f16 v[96:111], v[210:213], v[198:201], v[96:111]\n" \
  "v_pk_mul_f16 v189, v174, v178 op_sel:[0,0] op_sel_hi:[0,1]\n" \
  "v_pk_mul_f16 v193, v175, v179 op_sel:[0,0] op_sel_hi:[0,1]\n" \
  "v_mfma_f32_32x32x16_f16 v[112:127], v[214:217], v[198:201], v[112:127]\n" \
  "ds_read_b128 v[210:213], %[wl] offset:11616\n" \
  "ds_read_b128 v[214:217], %[wl] offset:34848\n" \
  "v_pk_mul_f16 v194, v176, v178 op_sel:[0,0] op_sel_hi:[0,1]\n" \
  "v_pk_mul_f16 v198, v177, v179 op_sel:[0,0] op_sel_hi:[0,1]\n" \
  "s_waitcnt lgkmcnt(3)\n" \
  "v_mfma_f32_32x32x16_f16 v[64:79], v[202:205], v[186:189], v[64:79]\n" \
  "v_pk_mul_f16 v195, v160, v180 op_sel:[0,0] op_sel_hi:[0,1]\n" \
  "v_pk_mul_f16 v199, v161, v181 op_sel:[0,0] op_sel_hi:[0,1]\n" \
  "s_waitcnt lgkmcnt(2)\n" \
  "v_mfma_f32_32x32x16_f16 v[80:95], v[206:209], v[186:189], v[80:95]\n" \
  "v_pk_mul_f16 v196, v162, v180 op_sel:[0,0] op_sel_hi:[0,1]\n" \
  "v_pk_mul_f16 v200, v163, v181 op_sel:[0,0] op_sel_hi:[0,1]\n" \
  "v_mfma_f32_32x32x16_f16 v[96:111], v[202:205], v[190:193], v[96:111]\n" \
  "v_pk_mul_f16 v197, v164, v180 op_sel:[0,0] op_sel_hi:[0,1]\n" \
  "v_pk_mul_f16 v201, v165, v181 op_sel:[0,0] op_sel_hi:[0,1]\n" \
  "v_mfma_f32_32x32x16_f16 v[112:127], v[206:209], v[190:193], v[112:127]\n" \
  "ds_read_b128 v[202:205], %[wl] offset:12672\n" \
  "ds_read_b128 v[206:209], %[wl] offset:35904\n" \
  "v_pk_mul_f16 v186, v166, v180 op_sel:[0,0] op_sel_hi:[0,1]\n" \
  "v_pk_mul_f16 v190, v167, v181 op_sel:[0,0] op_sel_hi:[0,1]\n" \
  "s_waitcnt lgkmcnt(3)\n" \
  "v_mfma_f32_32x32x16_f16 v[64:79], v[210:213], v[194:197], v[64:79]\n" \
  "v_pk_mul_f16 v187, v168, v180 op_sel:[0,0] op_sel_hi:[0,1]\n" \
  "v_pk_mul_f16 v191, v169, v181 op_sel:[0,0] op_sel_hi:[0,1]\n" \
  "s_waitcnt lgkmcnt(2)\n" \
  "v_mfma_f32_32x32x16_f16 v[80:95], v[214:217], v[194:197], v[80:95]\n" \
  "v_pk_mul_f16 v188, v170, v180 op_sel:[0,0] op_sel_hi:[0,1]\n" \
  "v_pk_mul_f16 v192, v171, v181 op_sel:[0,0] op_sel_hi:[0,1]\n" \
  "v_mfma_f32_32x32x16_f16 v[96:111], v[210:213], v[198:201], v[96:111]\n" \
  "v_pk_mul_f16 v189, v172, v180 op_sel:[0,0] op_sel_hi:[0,1]\n" \
  "v_pk_mul_f16 v193, v173, v181 op_sel:[0,0] op_sel_hi:[0,1]\n" \
  "v_mfma_f32_32x32x16_f16 v[112:127], v[214:217], v[198:201], v[112:127]\n" \
  "ds_read_b128 v[210:213], %[wl] offset:13728\n" \
  "ds_read_b128 v[214:217], %[wl] offset:36960\n" \
  "v_pk_mul_f16 v194, v174, v180 op_sel:[0,0] op_sel_hi:[0,1]\n" \
  "v_pk_mul_f16 v198, v175, v181 op_sel:[0,0] op_sel_hi:[0,1]\n" \
  "s_waitcnt lgkmcnt(3)\n" \
  "v_mfma_f32_32x32x16_f16 v[64:79], v[202:205], v[186:189], v[64:79]\n" \
  "v_pk_mul_f16 v195, v176, v180 op_sel:[0,0] op_sel_hi:[0,1]\n" \
  "v_pk_mul_f16 v199, v177, v181 op_sel:[0,0] op_sel_hi:[0,1]\n" \
  "s_waitcnt lgkmcnt(2)\n" \
  "v_mfma_f32_32x32x16_f16 v[80:95], v[206:209], v[186:189], v[80:95]\n" \
  "v_pk_mul_f16 v196, v178, v180 op_sel:[0,0] op_sel_hi:[0,1]\n" \
  "v_pk_mul_f16 v200, v179, v181 op_sel:[0,0] op_sel_hi:[0,1]\n" \
  "v_mfma_f32_32x32x16_f16 v[96:111], v[202:205], v[190:193], v[96:111]\n" \
  "v_pk_mul_f16 v197, v160, v182 op_sel:[0,0] op_sel_hi:[0,1]\n" \
  "v_pk_mul_f16 v201, v161, v183 op_sel:[0,0] op_sel_hi:[0,1]\n" \
  "v_mfma_f32_32x32x16_f16 v[112:127], v[206:209], v[190:193], v[112:127]\n" \
  "ds_read_b128 v[202:205], %[wl] offset:14784\n" \
  "ds_read_b128 v[206:209], %[wl] offset:38016\n" \
  "v_pk_mul_f16 v186, v162, v182 op_sel:[0,0] op_sel_hi:[0,1]\n" \
  "v_pk_mul_f16 v190, v163, v183 op_sel:[0,0] op_sel_hi:[0,1]\n" \
  "s_waitcnt lgkmcnt(3)\n" \
  "v_mfma_f32_32x32x16_f16 v[64:79], v[210:213], v[194:197], v[64:79]\n" \
  "v_pk_mul_f16 v187, v164, v182 op_sel:[0,0] op_sel_hi:[0,1]\n" \
  "v_pk_mul_f16 v191, v165, v183 op_sel:[0,0] op_sel_hi:[0,1]\n" \
  "s_waitcnt lgkmcnt(2)\n" \
  "v_mfma_f32_32x32x16_f16 v[80:95], v[214:217], v[194:197], v[80:95]\n" \
  "v_pk_mul_f16 v188, v166, v182 op_sel:[0,0] op_sel_hi:[0,1]\n" \
  "v_pk_mul_f16 v192, v167, v183 op_sel:[0,0] op_sel_hi:[0,1]\n" \
  "v_mfma_f32_32x32x16_f16 v[96:111], v[210:213], v[198:201], v[96:111]\n" \
  "v_pk_mul_f16 v189, v168, v182 op_sel:[0,0] op_sel_hi:[0,1]\n" \
  "v_pk_mul_f16 v193, v169, v183 op_sel:[0,0] op_sel_hi:[0,1]\n" \
  "v_mfma_f32_32x32x16_f16 v[112:127], v[214:217], v[198:201], v[112:127]\n" \
  "ds_read_b128 v[210:213], %[wl] offset:15840\n" \
  "ds_read_b128 v[214:217], %[wl] offset:39072\n" \
  "v_pk_mul_f16 v194, v170, v182 op_sel:[0,0] op_sel_hi:[0,1]\n" \
  "v_pk_mul_f16 v198, v171, v183 op_sel:[0,0] op_sel_hi:[0,1]\n" \
  "s_waitcnt lgkmcnt(3)\n" \
  "v_mfma_f32_32x32x16_f16 v[64:79], v[202:205], v[186:189], v[64:79]\n" \
  "v_pk_mul_f16 v195, v172, v182 op_sel:[0,0] op_sel_hi:[0,1]\n" \
  "v_pk_mul_f16 v199, v173, v183 op_sel:[0,0] op_sel_hi:[0,1]\n" \
  "s_waitcnt lgkmcnt(2)\n" \
  "v_mfma_f32_32x32x16_f16 v[80:95], v[206:209], v[186:189], v[80:95]\n" \
  "v_pk_mul_f16 v196, v174, v182 op_sel:[0,0] op_sel_hi:[0,1]\n" \
  "v_pk_mul_f16 v200, v175, v183 op_sel:[0,0] op_sel_hi:[0,1]\n" \
  "v_mfma_f32_32x32x16_f16 v[96:111], v[202:205], v[190:193], v[96:111]\n" \
  "v_pk_mul_f16 v197, v176, v182 op_sel:[0,0] op_sel_hi:[0,1]\n" \
  "v_pk_mul_f16 v201, v177, v183 op_sel:[0,0] op_sel_hi:[0,1]\n" \
  "v_mfma_f32_32x32x16_f16 v[112:127], v[206:209], v[190:193], v[112:127]\n" \
  "ds_read_b128 v[202:205], %[wl] offset:16896\n" \
  "ds_read_b128 v[206:209], %[wl] offset:40128\n" \
  "v_pk_mul_f16 v186, v178, v182 op_sel:[0,0] op_sel_hi:[0,1]\n" \
  "v_pk_mul_f16 v190, v179, v183 op_sel:[0,0] op_sel_hi:[0,1]\n" \
  "s_waitcnt lgkmcnt(3)\n" \
  "v_mfma_f32_32x32x16_f16 v[64:79], v[210:213], v[194:197], v[64:79]\n" \
  "v_pk_mul_f16 v187, v180, v182 op_sel:[0,0] op_sel_hi:[0,1]\n" \
  "v_pk_mul_f16 v191, v181, v183 op_sel:[0,0] op_sel_hi:[0,1]\n" \
  "s_waitcnt lgkmcnt(2)\n" \
  "v_mfma_f32_32x32x16_f16 v[80:95], v[214:217], v[194:197], v[80:95]\n" \
  "v_pk_mul_f16 v188, v160, v184 op_sel:[0,0] op_sel_hi:[0,1]\n" \
  "v_pk_mul_f16 v192, v161, v185 op_sel:[0,0] op_sel_hi:[0,1]\n" \
  "v_mfma_f32_32x32x16_f16 v[96:111], v[210:213], v[198:201], v[96:111]\n" \
  "v_pk_mul_f16 v189, v162, v184 op_sel:[0,0] op_sel_hi:[0,1]\n" \
  "v_pk_mul_f16 v193, v163, v185 op_sel:[0,0] op_sel_hi:[0,1]\n" \
  "v_mfma_f32_32x32x16_f16 v[112:127], v[214:217], v[198:201], v[112:127]\n" \
  "ds_read_b128 v[210:213], %[wl] offset:17952\n" \
  "ds_read_b128 v[214:217], %[wl] offset:41184\n" \
  "v_pk_mul_f16 v194, v164, v184 op_sel:[0,0] op_sel_hi:[0,1]\n" \
  "v_pk_mul_f16 v198, v165, v185 op_sel:[0,0] op_sel_hi:[0,1]\n" \
  "s_waitcnt lgkmcnt(3)\n" \
  "v_mfma_f32_32x32x16_f16 v[64:79], v[202:205], v[186:189], v[64:79]\n" \
  "v_pk_mul_f16 v195, v166, v184 op_sel:[0,0] op_sel_hi:[0,1]\n" \
  "v_pk_mul_f16 v199, v167, v185 op_sel:[0,0] op_sel_hi:[0,1]\n" \
  "s_waitcnt lgkmcnt(2)\n" \
  "v_mfma_f32_32x32x16_f16 v[80:95], v[206:209], v[186:189], v[80:95]\n" \
  "v_pk_mul_f16 v196, v168, v184 op_sel:[0,0] op_sel_hi:[0,1]\n" \
  "v_pk_mul_f16 v200, v169, v185 op_sel:[0,0] op_sel_hi:[0,1]\n" \
  "v_mfma_f32_32x32x16_f16 v[96:111], v[202:205], v[190:193], v[96:111]\n" \
  "v_pk_mul_f16 v197, v170, v184 op_sel:[0,0] op_sel_hi:[0,1]\n" \
  "v_pk_mul_f16 v201, v171, v185 op_sel:[0,0] op_sel_hi:[0,1]\n" \
  "v_mfma_f32_32x32x16_f16 v[112:127], v[206:209], v[190:193], v[112:127]\n" \
  "ds_read_b128 v[202:205], %[wl] offset:19008\n" \
  "ds_read_b128 v[206:209], %[wl] offset:42240\n" \
  "v_pk_mul_f16 v186, v172, v184 op_sel:[0,0] op_sel_hi:[0,1]\n" \
  "v_pk_mul_f16 v190, v173, v185 op_sel:[0,0] op_sel_hi:[0,1]\n" \
  "s_waitcnt lgkmcnt(3)\n" \
  "v_mfma_f32_32x32x16_f16 v[64:79], v[210:213], v[194:197], v[64:79]\n" \
  "v_pk_mul_f16 v187, v174, v184 op_sel:[0,0] op_sel_hi:[0,1]\n" \
  "v_pk_mul_f16 v191, v175, v185 op_sel:[0,0] op_sel_hi:[0,1]\n" \
  "s_waitcnt lgkmcnt(2)\n" \
  "v_mfma_f32_32x32x16_f16 v[80:95], v[214:217], v[194:197], v[80:95]\n" \
  "v_pk_mul_f16 v188, v176, v184 op_sel:[0,0] op_sel_hi:[0,1]\n" \
  "v_pk_mul_f16 v192, v177, v185 op_sel:[0,0] op_sel_hi:[0,1]\n" \
  "v_mfma_f32_32x32x16_f16 v[96:111], v[210:213], v[198:201], v[96:111]\n" \
  "v_pk_mul_f16 v189, v178, v184 op_sel:[0,0] op_sel_hi:[0,1]\n" \
  "v_pk_mul_f16 v193, v179, v185 op_sel:[0,0] op_sel_hi:[0,1]\n" \
  "v_mfma_f32_32x32x16_f16 v[112:127], v[214:217], v[198:201], v[112:127]\n" \
  "ds_read_b128 v[210:213], %[wl] offset:20064\n" \
  "ds_read_b128 v[214:217], %[wl] offset:43296\n" \
  "v_pk_mul_f16 v194, v180, v184 op_sel:[0,0] op_sel_hi:[0,1]\n" \
  "v_pk_mul_f16 v198, v181, v185 op_sel:[0,0] op_sel_hi:[0,1]\n" \
  "v_pk_mul_f16 v195, v182, v184 op_sel:[0,0] op_sel_hi:[0,1]\n" \
  "v_pk_mul_f16 v199, v183, v185 op_sel:[0,0] op_sel_hi:[0,1]\n" \
  "s_waitcnt lgkmcnt(3)\n" \
  "v_mfma_f32_32x32x16_f16 v[64:79], v[202:205], v[186:189], v[64:79]\n" \
  "v_pk_mul_f16 v196, v160, v160 op_sel:[0,0] op_sel_hi:[0,1]\n" \
  "v_pk_mul_f16 v222, v162, v162 op_sel:[0,0] op_sel_hi:[0,1]\n" \
  "v_bfi_b32 v196, %[hmv], v222, v196\n" \
  "v_pk_mul_f16 v200, v161, v161 op_sel:[0,0] op_sel_hi:[0,1]\n" \
  "s_waitcnt lgkmcnt(2)\n" \
  "v_mfma_f32_32x32x16_f16 v[80:95], v[206:209], v[186:189], v[80:95]\n" \
  "v_pk_mul_f16 v223, v163, v163 op_sel:[0,0] op_sel_hi:[0,1]\n" \
  "v_bfi_b32 v200, %[hmv], v223, v200\n" \
  "v_pk_mul_f16 v197, v164, v164 op_sel:[0,0] op_sel_hi:[0,1]\n" \
  "v_pk_mul_f16 v222, v166, v166 op_sel:[0,0] op_sel_hi:[0,1]\n" \
  "v_mfma_f32_32x32x16_f16 v[96:111], v[202:205], v[190:193], v[96:111]\n" \
  "v_bfi_b32 v197, %[hmv], v222, v197\n" \
  "v_pk_mul_f16 v201, v165, v165 op_sel:[0,0] op_sel_hi:[0,1]\n" \
  "v_pk_mul_f16 v223, v167, v167 op_sel:[0,0] op_sel_hi:[0,1]\n" \
  "v_bfi_b32 v201, %[hmv], v223, v201\n" \
  "v_mfma_f32_32x32x16_f16 v[112:127], v[206:209], v[190:193], v[112:127]\n" \
  "ds_read_b128 v[202:205], %[wl] offset:21120\n" \
  "ds_read_b128 v[206:209], %[wl] offset:44352\n" \
  "v_perm_b32 v222, v166, v162, %[psel]\n" \
  "v_pk_mul_f16 v186, v222, v222\n" \
  "v_perm_b32 v223, v167, v163, %[psel]\n" \
  "v_pk_mul_f16 v190, v223, v223\n" \
  "v_pk_mul_f16 v187, v168, v168 op_sel:[0,0] op_sel_hi:[0,1]\n" \
  "s_waitcnt lgkmcnt(3)\n" \
  "v_mfma_f32_32x32x16_f16 v[64:79], v[210:213], v[194:197], v[64:79]\n" \
  "v_pk_mul_f16 v222, v170, v170 op_sel:[0,0] op_sel_hi:[0,1]\n" \
  "v_bfi_b32 v187, %[hmv], v222, v187\n" \
  "v_pk_mul_f16 v191, v169, v169 op_sel:[0,0] op_sel_hi:[0,1]\n" \
  "v_pk_mul_f16 v223, v171, v171 op_sel:[0,0] op_sel_hi:[0,1]\n" \
  "v_bfi_b32 v191, %[hmv], v223, v191\n" \
  "s_waitcnt lgkmcnt(2)\n" \
  "v_mfma_f32_32x32x16_f16 v[80:95], v[214:217], v[194:197], v[80:95]\n" \
  "v_pk_mul_f16 v188, v172, v172 op_sel:[0,0] op_sel_hi:[0,1]\n" \
  "v_pk_mul_f16 v222, v174, v174 op_sel:[0,0] op_sel_hi:[0,1]\n" \
  "v_bfi_b32 v188, %[hmv], v222, v188\n" \
  "v_pk_mul_f16 v192, v173, v173 op_sel:[0,0] op_sel_hi:[0,1]\n" \
  "v_pk_mul_f16 v223, v175, v175 op_sel:[0,0] op_sel_hi:[0,1]\n" \
  "v_mfma_f32_32x32x16_f16 v[96:111], v[210:213], v[198:201], v[96:111]\n" \
  "v_bfi_b32 v192, %[hmv], v223, v192\n" \
  "v_perm_b32 v222, v174, v170, %[psel]\n" \
  "v_pk_mul_f16 v189, v222, v222\n" \
  "v_perm_b32 v223, v175, v171, %[psel]\n" \
  "v_pk_mul_f16 v193, v223, v223\n" \
  "v_mfma_f32_32x32x16_f16 v[112:127], v[214:217], v[198:201], v[112:127]\n" \
  "ds_read_b128 v[210:213], %[wl] offset:22176\n" \
  "ds_read_b128 v[214:217], %[wl] offset:45408\n" \
  "v_pk_mul_f16 v194, v176, v176 op_sel:[0,0] op_sel_hi:[0,1]\n" \
  "v_pk_mul_f16 v222, v178, v178 op_sel:[0,0] op_sel_hi:[0,1]\n" \
  "v_bfi_b32 v194, %[hmv], v222, v194\n" \
  "v_pk_mul_f16 v198, v177, v177 op_sel:[0,0] op_sel_hi:[0,1]\n" \
  "v_pk_mul_f16 v223, v179, v179 op_sel:[0,0] op_sel_hi:[0,1]\n" \
  "s_waitcnt lgkmcnt(3)\n" \
  "v_mfma_f32_32x32x16_f16 v[64:79], v[202:205], v[186:189], v[64:79]\n" \
  "v_bfi_b32 v198, %[hmv], v223, v198\n" \
  "v_pk_mul_f16 v195, v180, v180 op_sel:[0,0] op_sel_hi:[0,1]\n" \
  "v_pk_mul_f16 v222, v182, v182 op_sel:[0,0] op_sel_hi:[0,1]\n" \
  "v_bfi_b32 v195, %[hmv], v222, v195\n" \
  "v_pk_mul_f16 v199, v181, v181 op_sel:[0,0] op_sel_hi:[0,1]\n" \
  "s_waitcnt lgkmcnt(2)\n" \
  "v_mfma_f32_32x32x16_f16 v[80:95], v[206:209], v[186:189], v[80:95]\n" \
  "v_pk_mul_f16 v223, v183, v183 op_sel:[0,0] op_sel_hi:[0,1]\n" \
  "v_bfi_b32 v199, %[hmv], v223, v199\n" \
  "v_perm_b32 v222, v182, v178, %[psel]\n" \
  "v_pk_mul_f16 v196, v222, v222\n" \
  "v_mfma_f32_32x32x16_f16 v[96:111], v[202:205], v[190:193], v[96:111]\n" \
  "v_perm_b32 v223, v183, v179, %[psel]\n" \
  "v_pk_mul_f16 v200, v223, v223\n" \
  "v_pk_mul_f16 v197, v184, v184 op_sel:[0,0] op_sel_hi:[0,1]\n" \
  "v_pk_mul_f16 v201, v185, v185 op_sel:[0,0] op_sel_hi:[0,1]\n" \
  "v_mfma_f32_32x32x16_f16 v[112:127], v[206:209], v[190:193], v[112:127]\n" \
  "s_waitcnt lgkmcnt(1)\n" \
  "v_mfma_f32_32x32x16_f16 v[64:79], v[210:213], v[194:197], v[64:79]\n" \
  "s_waitcnt lgkmcnt(0)\n" \
  "v_mfma_f32_32x32x16_f16 v[80:95], v[214:217], v[194:197], v[80:95]\n" \
  "v_mfma_f32_32x32x16_f16 v[96:111], v[210:213], v[198:201], v[96:111]\n" \
  "v_mfma_f32_32x32x16_f16 v[112:127], v[214:217], v[198:201], v[112:127]\n" \
  "s_setprio 0\n" \
  "v_lshlrev_b32 v221, 1, v221\n" \
  "v_sub_u32 v220, v221, %[k1]\n" \
  "v_add_u32 v221, v221, v220\n" \
  "v_subrev_u32 v221, %[k2], v221\n" \
  "s_nop 5\n" \
  "v_mul_f32 v218, v64, v80\n" \
  "v_mul_f32 v219, v64, v128\n" \
  "v_fmac_f32 v218, v65, v81\n" \
  "v_fmac_f32 v219, v65, v129\n" \
  "v_fmac_f32 v218, v66, v82\n" \
  "v_fmac_f32 v219, v66, v130\n" \
  "v_fmac_f32 v218, v67, v83\n" \
  "v_fmac_f32 v219, v67, v131\n" \
  "v_fmac_f32 v218, v68, v84\n" \
  "v_fmac_f32 v219, v68, v132\n" \
  "v_fmac_f32 v218, v69, v85\n" \
  "v_fmac_f32 v219, v69, v133\n" \
  "v_fmac_f32 v218, v70, v86\n" \
  "v_fmac_f32 v219, v70, v134\n" \
  "v_fmac_f32 v218, v71, v87\n" \
  "v_fmac_f32 v219, v71, v135\n" \
  "v_fmac_f32 v218, v72, v88\n" \
  "v_fmac_f32 v219, v72, v136\n" \
  "v_fmac_f32 v218, v73, v89\n" \
  "v_fmac_f32 v219, v73, v137\n" \
  "v_fmac_f32 v218, v74, v90\n" \
  "v_fmac_f32 v219, v74, v138\n" \
  "v_fmac_f32 v218, v75, v91\n" \
  "v_fmac_f32 v219, v75, v139\n" \
  "v_mul_f32 v222, v76, v92\n" \
  "v_mul_f32 v223, v76, v140\n" \
  "v_fmac_f32 v222, v77, v93\n" \
  "v_fmac_f32 v223, v77, v141\n" \
  "v_fmac_f32 v219, v96, v144\n" \
  "v_fmac_f32 v218, v96, v112\n" \
  "v_fmac_f32 v219, v97, v145\n" \
  "v_fmac_f32 v218, v97, v113\n" \
  "v_fmac_f32 v219, v98, v146\n" \
  "v_fmac_f32 v218, v98, v114\n" \
  "v_fmac_f32 v219, v99, v147\n" \
  "v_fmac_f32 v218, v99, v115\n" \
  "v_fmac_f32 v219, v100, v148\n" \
  "v_fmac_f32 v218, v100, v116\n" \
  "v_fmac_f32 v219, v101, v149\n" \
  "v_fmac_f32 v218, v101, v117\n" \
  "v_fmac_f32 v219, v102, v150\n" \
  "v_fmac_f32 v218, v102, v118\n" \
  "v_fmac_f32 v219, v103, v151\n" \
  "v_fmac_f32 v218, v103, v119\n" \
  "v_fmac_f32 v219, v104, v152\n" \
  "v_fmac_f32 v218, v104, v120\n" \
  "v_fmac_f32 v219, v105, v153\n" \
  "v_fmac_f32 v218, v105, v121\n" \
  "v_fmac_f32 v219, v106, v154\n" \
  "v_fmac_f32 v218, v106, v122\n" \
  "v_fmac_f32 v219, v107, v155\n" \
  "v_fmac_f32 v218, v107, v123\n" \
  "v_fmac_f32 v222, v108, v124\n" \
  "v_fmac_f32 v223, v108, v156\n" \
  "v_fmac_f32 v222, v109, v125\n" \
  "v_fmac_f32 v223, v109, v157\n" \
  "v_cndmask_b32 v222, v222, 0, %[hm]\n" \
  "v_cndmask_b32 v223, v223, 0, %[hm]\n" \
  "v_add_f32 v218, v218, v222\n" \
  "v_add_f32 v219, v219, v223\n" \
  "v_ldexp_f32 v218, v218, v221\n" \
  "v_ldexp_f32 v219, v219, v220\n" \
  "v_add_f32 v218, v218, v219\n" \
  "s_nop 1\n" \
  "v_add_f32_dpp v218, v218, v218 row_shr:1\n" \
  "s_nop 1\n" \
  "v_add_f32_dpp v218, v218, v218 row_shr:2\n" \
  "s_nop 1\n" \
  "v_add_f32_dpp v218, v218, v218 row_shr:4\n" \
  "s_nop 1\n" \
  "v_add_f32_dpp v218, v218, v218 row_shr:8\n" \
  "s_nop 1\n" \
  "v_add_f32_dpp v218, v218, v218 row_bcast:15 row_mask:0xa\n" \
  "s_nop 1\n" \
  "v_add_f32_dpp v218, v218, v218 row_bcast:31 row_mask:0xc\n" \
  "v_mov_b32 %[res], v218\n"
#define CIN_UNIT_CLOB "v64", "v65", "v66", "v67", "v68", "v69", "v70", "v71", "v72", "v73", "v74", "v75", "v76", "v77", "v78", "v79", "v80", "v81", "v82", "v83", "v84", "v85", "v86", "v87", "v88", "v89", "v90", "v91", "v92", "v93", "v94", "v95", "v96", "v97", "v98", "v99", "v100", "v101", "v102", "v103", "v104", "v105", "v106", "v107", "v108", "v109", "v110", "v111", "v112", "v113", "v114", "v115", "v116", "v117", "v118", "v119", "v120", "v121", "v122", "v123", "v124", "v125", "v126", "v127", "v128", "v129", "v130", "v131", "v132", "v133", "v134", "v135", "v136", "v137", "v138", "v139", "v140", "v141", "v142", "v143", "v144", "v145", "v146", "v147", "v148", "v149", "v150", "v151", "v152", "v153", "v154", "v155", "v156", "v157", "v158", "v159", "v160", "v161", "v162", "v163", "v164", "v165", "v166", "v167", "v168", "v169", "v170", "v171", "v172", "v173", "v174", "v175", "v176", "v177", "v178", "v179", "v180", "v181", "v182", "v183", "v184", "v185", "v186", "v187", "v188", "v189", "v190", "v191", "v192", "v193", "v194", "v195", "v196", "v197", "v198", "v199", "v200", "v201", "v202", "v203", "v204", "v205", "v206", "v207", "v208", "v209", "v210", "v211", "v212", "v213", "v214", "v215", "v216", "v217", "v218", "v219", "v220", "v221", "v222", "v223"
#define CIN_TABLE_INIT { \
  0x00340002u, 0x02d8001cu, 0x00680004u, 0x030c001eu, 0x006a0038u, 0x030e0052u, 0x009c0006u, 0x03400020u, 0x02f202c0u, 0x004e02a6u, 0x032602c2u, 0x008202a8u, 0x032802f6u, 0x008402dcu, 0x035a02c4u, 0x00b602aau, \
  0x009e003au, 0x03420054u, 0x00a0006eu, 0x03440088u, 0x00d00008u, 0x03740022u, 0x00d2003cu, 0x03760056u, 0x035c02f8u, 0x00b802deu, 0x035e032cu, 0x00ba0312u, 0x038e02c6u, 0x00ea02acu, 0x039002fau, 0x00ec02e0u, \
  0x00d40070u, 0x0378008au, 0x00d600a4u, 0x037a00beu, 0x0104000au, 0x03a80024u, 0x0106003eu, 0x03aa0058u, 0x0392032eu, 0x00ee0314u, 0x03940362u, 0x00f00348u, 0x03c202c8u, 0x011e02aeu, 0x03c402fcu, 0x012002e2u, \
  0x01080072u, 0x03ac008cu, 0x010a00a6u, 0x03ae00c0u, 0x010c00dau, 0x03b000f4u, 0x0138000cu, 0x03dc0026u, 0x03c60330u, 0x01220316u, 0x03c80364u, 0x0124034au, 0x03ca0398u, 0x0126037eu, 0x03f602cau, 0x015202b0u, \
  0x013a0040u, 0x03de005au, 0x013c0074u, 0x03e0008eu, 0x013e00a8u, 0x03e200c2u, 0x014000dcu, 0x03e400f6u, 0x03f802feu, 0x015402e4u, 0x03fa0332u, 0x01560318u, 0x03fc0366u, 0x0158034cu, 0x03fe039au, 0x015a0380u, \
  0x01420110u, 0x03e6012au, 0x016c000eu, 0x04100028u, 0x016e0042u, 0x0412005cu, 0x01700076u, 0x04140090u, 0x040003ceu, 0x015c03b4u, 0x042a02ccu, 0x018602b2u, 0x042c0300u, 0x018802e6u, 0x042e0334u, 0x018a031au, \
  0x017200aau, 0x041600c4u, 0x017400deu, 0x041800f8u, 0x01760112u, 0x041a012cu, 0x01780146u, 0x041c0160u, 0x04300368u, 0x018c034eu, 0x0432039cu, 0x018e0382u, 0x043403d0u, 0x019003b6u, 0x04360404u, 0x019203eau, \
  0x01a00010u, 0x0444002au, 0x01a20044u, 0x0446005eu, 0x01a40078u, 0x04480092u, 0x01a600acu, 0x044a00c6u, 0x045e02ceu, 0x01ba02b4u, 0x04600302u, 0x01bc02e8u, 0x04620336u, 0x01be031cu, 0x0464036au, 0x01c00350u, \
  0x01a800e0u, 0x044c00fau, 0x01aa0114u, 0x044e012eu, 0x01ac0148u, 0x04500162u, 0x01ae017cu, 0x04520196u, 0x0466039eu, 0x01c20384u, 0x046803d2u, 0x01c403b8u, 0x046a0406u, 0x01c603ecu, 0x046c043au, 0x01c80420u, \
  0x01d40012u, 0x0478002cu, 0x01d60046u, 0x047a0060u, 0x01d8007au, 0x047c0094u, 0x01da00aeu, 0x047e00c8u, 0x049202d0u, 0x01ee02b6u, 0x04940304u, 0x01f002eau, 0x04960338u, 0x01f2031eu, 0x0498036cu, 0x01f40352u, \
  0x01dc00e2u, 0x048000fcu, 0x01de0116u, 0x04820130u, 0x01e0014au, 0x04840164u, 0x01e2017eu, 0x04860198u, 0x049a03a0u, 0x01f60386u, 0x049c03d4u, 0x01f803bau, 0x049e0408u, 0x01fa03eeu, 0x04a0043cu, 0x01fc0422u, \
  0x01e401b2u, 0x048801ccu, 0x02080014u, 0x04ac002eu, 0x020a0048u, 0x04ae0062u, 0x020c007cu, 0x04b00096u, 0x04a20470u, 0x01fe0456u, 0x04c602d2u, 0x022202b8u, 0x04c80306u, 0x022402ecu, 0x04ca033au, 0x02260320u, \
  0x020e00b0u, 0x04b200cau, 0x021000e4u, 0x04b400feu, 0x02120118u, 0x04b60132u, 0x0214014cu, 0x04b80166u, 0x04cc036eu, 0x02280354u, 0x04ce03a2u, 0x022a0388u, 0x04d003d6u, 0x022c03bcu, 0x04d2040au, 0x022e03f0u, \
  0x02160180u, 0x04ba019au, 0x021801b4u, 0x04bc01ceu, 0x021a01e8u, 0x04be0202u, 0x023c0016u, 0x04e00030u, 0x04d4043eu, 0x02300424u, 0x04d60472u, 0x02320458u, 0x04d804a6u, 0x0234048cu, 0x04fa02d4u, 0x025602bau, \
  0x023e004au, 0x04e20064u, 0x0240007eu, 0x04e40098u, 0x024200b2u, 0x04e600ccu, 0x024400e6u, 0x04e80100u, 0x04fc0308u, 0x025802eeu, 0x04fe033cu, 0x025a0322u, 0x05000370u, 0x025c0356u, 0x050203a4u, 0x025e038au, \
  0x0246011au, 0x04ea0134u, 0x0248014eu, 0x04ec0168u, 0x024a0182u, 0x04ee019cu, 0x024c01b6u, 0x04f001d0u, 0x050403d8u, 0x026003beu, 0x0506040cu, 0x026203f2u, 0x05080440u, 0x02640426u, 0x050a0474u, 0x0266045au, \
  0x024e01eau, 0x04f20204u, 0x0250021eu, 0x04f40238u, 0x02700018u, 0x05140032u, 0x0272004cu, 0x05160066u, 0x050c04a8u, 0x0268048eu, 0x050e04dcu, 0x026a04c2u, 0x052e02d6u, 0x028a02bcu, 0x0530030au, 0x028c02f0u, \
  0x02740080u, 0x0518009au, 0x027600b4u, 0x051a00ceu, 0x027800e8u, 0x051c0102u, 0x027a011cu, 0x051e0136u, 0x0532033eu, 0x028e0324u, 0x05340372u, 0x02900358u, 0x053603a6u, 0x0292038cu, 0x053803dau, 0x029403c0u, \
  0x027c0150u, 0x0520016au, 0x027e0184u, 0x0522019eu, 0x028001b8u, 0x052401d2u, 0x028201ecu, 0x05260206u, 0x053a040eu, 0x029603f4u, 0x053c0442u, 0x02980428u, 0x053e0476u, 0x029a045cu, 0x054004aau, 0x029c0490u, \
  0x02840220u, 0x0528023au, 0x02860254u, 0x052a026eu, 0x80000000u, 0x02a4001au, 0x806c006cu, 0x03100086u, 0x054204deu, 0x029e04c4u, 0x05440512u, 0x02a004f8u, 0x82be02beu, 0x02da0050u, 0x832a032au, 0x034600bcu, \
  0x80360036u, 0x80a200a2u, 0x80d800d8u, 0x037c00f2u, 0x81440144u, 0x03e8015eu, 0x810e010eu, 0x817a017au, 0x82f402f4u, 0x83600360u, 0x83960396u, 0x03b20128u, 0x84020402u, 0x041e0194u, 0x83cc03ccu, 0x84380438u, \
  0x81b001b0u, 0x045401cau, 0x821c021cu, 0x04c00236u, 0x81e601e6u, 0x82520252u, 0x82880288u, 0x852c02a2u, 0x846e046eu, 0x048a0200u, 0x84da04dau, 0x04f6026cu, 0x84a404a4u, 0x85100510u, 0x85460546u, 0x82a2052cu, \
  0x80000000u, 0x80000000u, 0x80000000u, 0x80000000u, 0x80000000u, 0x80000000u, 0x80000000u, 0x80000000u, 0x80000000u, 0x80000000u, 0x80000000u, 0x80000000u, 0x80000000u, 0x80000000u, 0x80000000u, 0x80000000u, \
  0x80000000u, 0x80000000u, 0x80000000u, 0x80000000u, 0x80000000u, 0x80000000u, 0x80000000u, 0x80000000u, 0x80000000u, 0x80000000u, 0x80000000u, 0x80000000u, 0x80000000u, 0x80000000u, 0x80000000u, 0x80000000u, \
  0x00350002u, 0x02e6001cu, 0x006a0004u, 0x031b001eu, 0x006c0039u, 0x031d0053u, 0x009f0006u, 0x03500020u, 0x030002cdu, 0x004f02b3u, 0x033502cfu, 0x008402b5u, 0x03370304u, 0x008602eau, 0x036a02d1u, 0x00b902b7u, \
  0x00a1003bu, 0x03520055u, 0x00a30070u, 0x0354008au, 0x00d40008u, 0x03850022u, 0x00d6003du, 0x03870057u, 0x036c0306u, 0x00bb02ecu, 0x036e033bu, 0x00bd0321u, 0x039f02d3u, 0x00ee02b9u, 0x03a10308u, 0x00f002eeu, \
  0x00d80072u, 0x0389008cu, 0x00da00a7u, 0x038b00c1u, 0x0109000au, 0x03ba0024u, 0x010b003fu, 0x03bc0059u, 0x03a3033du, 0x00f20323u, 0x03a50372u, 0x00f40358u, 0x03d402d5u, 0x012302bbu, 0x03d6030au, 0x012502f0u, \
  0x010d0074u, 0x03be008eu, 0x010f00a9u, 0x03c000c3u, 0x011100deu, 0x03c200f8u, 0x013e000cu, 0x03ef0026u, 0x03d8033fu, 0x01270325u, 0x03da0374u, 0x0129035au, 0x03dc03a9u, 0x012b038fu, 0x040902d7u, 0x015802bdu, \
  0x01400041u, 0x03f1005bu, 0x01420076u, 0x03f30090u, 0x014400abu, 0x03f500c5u, 0x014600e0u, 0x03f700fau, 0x040b030cu, 0x015a02f2u, 0x040d0341u, 0x015c0327u, 0x040f0376u, 0x015e035cu, 0x041103abu, 0x01600391u, \
  0x01480115u, 0x03f9012fu, 0x0173000eu, 0x04240028u, 0x01750043u, 0x0426005du, 0x01770078u, 0x04280092u, 0x041303e0u, 0x016203c6u, 0x043e02d9u, 0x018d02bfu, 0x0440030eu, 0x018f02f4u, 0x04420343u, 0x01910329u, \
  0x017900adu, 0x042a00c7u, 0x017b00e2u, 0x042c00fcu, 0x017d0117u, 0x042e0131u, 0x017f014cu, 0x04300166u, 0x04440378u, 0x0193035eu, 0x044603adu, 0x01950393u, 0x044803e2u, 0x019703c8u, 0x044a0417u, 0x019903fdu, \
  0x01a80010u, 0x0459002au, 0x01aa0045u, 0x045b005fu, 0x01ac007au, 0x045d0094u, 0x01ae00afu, 0x045f00c9u, 0x047302dbu, 0x01c202c1u, 0x04750310u, 0x01c402f6u, 0x04770345u, 0x01c6032bu, 0x0479037au, 0x01c80360u, \
  0x01b000e4u, 0x046100feu, 0x01b20119u, 0x04630133u, 0x01b4014eu, 0x04650168u, 0x01b60183u, 0x0467019du, 0x047b03afu, 0x01ca0395u, 0x047d03e4u, 0x01cc03cau, 0x047f0419u, 0x01ce03ffu, 0x0481044eu, 0x01d00434u, \
  0x01dd0012u, 0x048e002cu, 0x01df0047u, 0x04900061u, 0x01e1007cu, 0x04920096u, 0x01e300b1u, 0x049400cbu, 0x04a802ddu, 0x01f702c3u, 0x04aa0312u, 0x01f902f8u, 0x04ac0347u, 0x01fb032du, 0x04ae037cu, 0x01fd0362u, \
  0x01e500e6u, 0x04960100u, 0x01e7011bu, 0x04980135u, 0x01e90150u, 0x049a016au, 0x01eb0185u, 0x049c019fu, 0x04b003b1u, 0x01ff0397u, 0x04b203e6u, 0x020103ccu, 0x04b4041bu, 0x02030401u, 0x04b60450u, 0x02050436u, \
  0x01ed01bau, 0x049e01d4u, 0x02120014u, 0x04c3002eu, 0x02140049u, 0x04c50063u, 0x0216007eu, 0x04c70098u, 0x04b80485u, 0x0207046bu, 0x04dd02dfu, 0x022c02c5u, 0x04df0314u, 0x022e02fau, 0x04e10349u, 0x0230032fu, \
  0x021800b3u, 0x04c900cdu, 0x021a00e8u, 0x04cb0102u, 0x021c011du, 0x04cd0137u, 0x021e0152u, 0x04cf016cu, 0x04e3037eu, 0x02320364u, 0x04e503b3u, 0x02340399u, 0x04e703e8u, 0x023603ceu, 0x04e9041du, 0x02380403u, \
  0x02200187u, 0x04d101a1u, 0x022201bcu, 0x04d301d6u, 0x022401f1u, 0x04d5020bu, 0x02470016u, 0x04f80030u, 0x04eb0452u, 0x023a0438u, 0x04ed0487u, 0x023c046du, 0x04ef04bcu, 0x023e04a2u, 0x051202e1u, 0x026102c7u, \
  0x0249004bu, 0x04fa0065u, 0x024b0080u, 0x04fc009au, 0x024d00b5u, 0x04fe00cfu, 0x024f00eau, 0x05000104u, 0x05140316u, 0x026302fcu, 0x0516034bu, 0x02650331u, 0x05180380u, 0x02670366u, 0x051a03b5u, 0x0269039bu, \
  0x0251011fu, 0x05020139u, 0x02530154u, 0x0504016eu, 0x02550189u, 0x050601a3u, 0x025701beu, 0x050801d8u, 0x051c03eau, 0x026b03d0u, 0x051e041fu, 0x026d0405u, 0x05200454u, 0x026f043au, 0x05220489u, 0x0271046fu, \
  0x025901f3u, 0x050a020du, 0x025b0228u, 0x050c0242u, 0x027c0018u, 0x052d0032u, 0x027e004du, 0x052f0067u, 0x052404beu, 0x027304a4u, 0x052604f3u, 0x027504d9u, 0x054702e3u, 0x029602c9u, 0x05490318u, 0x029802feu, \
  0x02800082u, 0x0531009cu, 0x028200b7u, 0x053300d1u, 0x028400ecu, 0x05350106u, 0x02860121u, 0x0537013bu, 0x054b034du, 0x029a0333u, 0x054d0382u, 0x029c0368u, 0x054f03b7u, 0x029e039du, 0x055103ecu, 0x02a003d2u, \
  0x02880156u, 0x05390170u, 0x028a018bu, 0x053b01a5u, 0x028c01c0u, 0x053d01dau, 0x028e01f5u, 0x053f020fu, 0x05530421u, 0x02a20407u, 0x05550456u, 0x02a4043cu, 0x0557048bu, 0x02a60471u, 0x055904c0u, 0x02a804a6u, \
  0x0290022au, 0x05410244u, 0x0292025fu, 0x05430279u, 0x80000000u, 0x02b1001au, 0x806e006eu, 0x031f0088u, 0x055b04f5u, 0x02aa04dbu, 0x055d052au, 0x02ac0510u, 0x82cb02cbu, 0x02e80051u, 0x83390339u, 0x035600bfu, \
  0x80370037u, 0x80a500a5u, 0x80dc00dcu, 0x038d00f6u, 0x814a014au, 0x03fb0164u, 0x81130113u, 0x81810181u, 0x83020302u, 0x83700370u, 0x83a703a7u, 0x03c4012du, 0x84150415u, 0x0432019bu, 0x83de03deu, 0x844c044cu, \
  0x81b801b8u, 0x046901d2u, 0x82260226u, 0x04d70240u, 0x81ef01efu, 0x825d025du, 0x82940294u, 0x854502aeu, 0x84830483u, 0x04a00209u, 0x84f104f1u, 0x050e0277u, 0x84ba04bau, 0x85280528u, 0x855f055fu, 0x82ae0545u, \
  0x80000000u, 0x80000000u, 0x80000000u, 0x80000000u, 0x80000000u, 0x80000000u, 0x80000000u, 0x80000000u, 0x80000000u, 0x80000000u, 0x80000000u, 0x80000000u, 0x80000000u, 0x80000000u, 0x80000000u, 0x80000000u, \
  0x80000000u, 0x80000000u, 0x80000000u, 0x80000000u, 0x80000000u, 0x80000000u, 0x80000000u, 0x80000000u, 0x80000000u, 0x80000000u, 0x80000000u, 0x80000000u, 0x80000000u, 0x80000000u, 0x80000000u, 0x80000000u, }
#define GEMM_CONS_ASM \
  "s_barrier\n" \
  "ds_read_b64_tr_b16 v[192:193], v48 offset:0\n" \
  "ds_read_b64_tr_b16 v[194:195], v49 offset:0\n" \
  "ds_read_b128 v[224:227], v56 offset:0\n" \
  "ds_read_b64_tr_b16 v[196:197], v50 offset:0\n" \
  "ds_read_b64_tr_b16 v[198:199], v51 offset:0\n" \
  "ds_read_b64_tr_b16 v[200:201], v52 offset:0\n" \
  "ds_read_b64_tr_b16 v[202:203], v53 offset:0\n" \
  "ds_read_b64_tr_b16 v[204:205], v54 offset:0\n" \
  "ds_read_b64_tr_b16 v[206:207], v55 offset:0\n" \
  "ds_read_b128 v[228:231], v56 offset:2048\n" \
  "ds_read_b128 v[232:235], v56 offset:4096\n" \
  "ds_read_b128 v[236:239], v56 offset:6144\n" \
  "ds_read_b128 v[240:243], v57 offset:0\n" \
  "ds_read_b128 v[244:247], v57 offset:2048\n" \
  "1:\n" \
  "s_waitcnt lgkmcnt(11)\n" \
  "v_mfma_f32_16x16x32_bf16 v[64:67], v[192:195], v[224:227], v[64:67]\n" \
  "ds_read_b128 v[248:251], v57 offset:4096\n" \
  "s_waitcnt lgkmcnt(10)\n" \
  "v_mfma_f32_16x16x32_bf16 v[68:71], v[196:199], v[224:227], v[68:71]\n" \
  "s_waitcnt lgkmcnt(8)\n" \
  "v_mfma_f32_16x16x32_bf16 v[72:75], v[200:203], v[224:227], v[72:75]\n" \
  "s_waitcnt lgkmcnt(6)\n" \
  "v_mfma_f32_16x16x32_bf16 v[76:79], v[204:207], v[224:227], v[76:79]\n" \
  "s_waitcnt lgkmcnt(5)\n" \
  "v_mfma_f32_16x16x32_bf16 v[92:95], v[204:207], v[228:231], v[92:95]\n" \
  "ds_read_b128 v[252:255], v57 offset:6144\n" \
  "v_mfma_f32_16x16x32_bf16 v[88:91], v[200:203], v[228:231], v[88:91]\n" \
  "ds_read_b64_tr_b16 v[208:209], v48 offset:8192\n" \
  "v_mfma_f32_16x16x32_bf16 v[84:87], v[196:199], v[228:231], v[84:87]\n" \
  "ds_read_b64_tr_b16 v[210:211], v49 offset:8192\n" \
  "v_mfma_f32_16x16x32_bf16 v[80:83], v[192:195], v[228:231], v[80:83]\n" \
  "s_waitcnt lgkmcnt(7)\n" \
  "v_mfma_f32_16x16x32_bf16 v[96:99], v[192:195], v[232:235], v[96:99]\n" \
  "ds_read_b128 v[224:227], v58 offset:0\n" \
  "v_mfma_f32_16x16x32_bf16 v[100:103], v[196:199], v[232:235], v[100:103]\n" \
  "ds_read_b64_tr_b16 v[212:213], v50 offset:8192\n" \
  "v_mfma_f32_16x16x32_bf16 v[104:107], v[200:203], v[232:235], v[104:107]\n" \
  "ds_read_b64_tr_b16 v[214:215], v51 offset:8192\n" \
  "v_mfma_f32_16x16x32_bf16 v[108:111], v[204:207], v[232:235], v[108:111]\n" \
  "s_waitcnt lgkmcnt(9)\n" \
  "v_mfma_f32_16x16x32_bf16 v[124:127], v[204:207], v[236:239], v[124:127]\n" \
  "ds_read_b128 v[228:231], v58 offset:2048\n" \
  "v_mfma_f32_16x16x32_bf16 v[120:123], v[200:203], v[236:239], v[120:123]\n" \
  "ds_read_b64_tr_b16 v[216:217], v52 offset:8192\n" \
  "v_mfma_f32_16x16x32_bf16 v[116:119], v[196:199], v[236:239], v[116:119]\n" \
  "ds_read_b64_tr_b16 v[218:219], v53 offset:8192\n" \
  "v_mfma_f32_16x16x32_bf16 v[112:115], v[192:195], v[236:239], v[112:115]\n" \
  "s_waitcnt lgkmcnt(11)\n" \
  "v_mfma_f32_16x16x32_bf16 v[128:131], v[192:195], v[240:243], v[128:131]\n" \
  "ds_read_b128 v[232:235], v58 offset:4096\n" \
  "v_mfma_f32_16x16x32_bf16 v[132:135], v[196:199], v[240:243], v[132:135]\n" \
  "ds_read_b64_tr_b16 v[220:221], v54 offset:8192\n" \
  "v_mfma_f32_16x16x32_bf16 v[136:139], v[200:203], v[240:243], v[136:139]\n" \
  "ds_read_b64_tr_b16 v[222:223], v55 offset:8192\n" \
  "v_mfma_f32_16x16x32_bf16 v[140:143], v[204:207], v[240:243], v[140:143]\n" \
  "s_waitcnt lgkmcnt(13)\n" \
  "v_mfma_f32_16x16x32_bf16 v[156:159], v[204:207], v[244:247], v[156:159]\n" \
  "ds_read_b128 v[236:239], v58 offset:6144\n" \
  "s_mov_b32 %[dl], 0xc000\n" \
  "v_mfma_f32_16x16x32_bf16 v[152:155], v[200:203], v[244:247], v[152:155]\n" \
  "s_add_u32 %[ph], %[ph], 1\n" \
  "s_cmp_eq_u32 %[ph], 3\n" \
  "v_mfma_f32_16x16x32_bf16 v[148:151], v[196:199], v[244:247], v[148:151]\n" \
  "s_cselect_b32 %[dl], 0xfffe8000, %[dl]\n" \
  "s_cselect_b32 %[ph], 0, %[ph]\n" \
  "v_mfma_f32_16x16x32_bf16 v[144:147], v[192:195], v[244:247], v[144:147]\n" \
  "s_waitcnt lgkmcnt(13)\n" \
  "v_mfma_f32_16x16x32_bf16 v[160:163], v[192:195], v[248:251], v[160:163]\n" \
  "ds_read_b128 v[240:243], v59 offset:0\n" \
  "v_add_u32 v48, %[dl], v48\n" \
  "v_mfma_f32_16x16x32_bf16 v[164:167], v[196:199], v[248:251], v[164:167]\n" \
  "v_add_u32 v49, %[dl], v49\n" \
  "v_mfma_f32_16x16x32_bf16 v[168:171], v[200:203], v[248:251], v[168:171]\n" \
  "v_add_u32 v50, %[dl], v50\n" \
  "v_mfma_f32_16x16x32_bf16 v[172:175], v[204:207], v[248:251], v[172:175]\n" \
  "v_add_u32 v51, %[dl], v51\n" \
  "s_waitcnt lgkmcnt(13)\n" \
  "v_mfma_f32_16x16x32_bf16 v[188:191], v[204:207], v[252:255], v[188:191]\n" \
  "ds_read_b128 v[244:247], v59 offset:2048\n" \
  "v_add_u32 v52, %[dl], v52\n" \
  "v_mfma_f32_16x16x32_bf16 v[184:187], v[200:203], v[252:255], v[184:187]\n" \
  "v_add_u32 v53, %[dl], v53\n" \
  "v_mfma_f32_16x16x32_bf16 v[180:183], v[196:199], v[252:255], v[180:183]\n" \
  "v_add_u32 v54, %[dl], v54\n" \
  "v_mfma_f32_16x16x32_bf16 v[176:179], v[192:195], v[252:255], v[176:179]\n" \
  "v_add_u32 v55, %[dl], v55\n" \
  "s_waitcnt lgkmcnt(11)\n" \
  "v_mfma_f32_16x16x32_bf16 v[64:67], v[208:211], v[224:227], v[64:67]\n" \
  "ds_read_b128 v[248:251], v59 offset:4096\n" \
  "s_waitcnt lgkmcnt(10)\n" \
  "v_mfma_f32_16x16x32_bf16 v[68:71], v[212:215], v[224:227], v[68:71]\n" \
  "s_waitcnt lgkmcnt(7)\n" \
  "v_mfma_f32_16x16x32_bf16 v[72:75], v[216:219], v[224:227], v[72:75]\n" \
  "s_waitcnt lgkmcnt(4)\n" \
  "v_mfma_f32_16x16x32_bf16 v[76:79], v[220:223], v[224:227], v[76:79]\n" \
  "v_mfma_f32_16x16x32_bf16 v[92:95], v[220:223], v[228:231], v[92:95]\n" \
  "ds_read_b128 v[252:255], v59 offset:6144\n" \
  "v_mfma_f32_16x16x32_bf16 v[88:91], v[216:219], v[228:231], v[88:91]\n" \
  "v_mfma_f32_16x16x32_bf16 v[84:87], v[212:215], v[228:231], v[84:87]\n" \
  "v_mfma_f32_16x16x32_bf16 v[80:83], v[208:211], v[228:231], v[80:83]\n" \
  "v_mfma_f32_16x16x32_bf16 v[96:99], v[208:211], v[232:235], v[96:99]\n" \
  "v_add_u32 v56, %[dl], v56\n" \
  "v_mfma_f32_16x16x32_bf16 v[100:103], v[212:215], v[232:235], v[100:103]\n" \
  "v_add_u32 v57, %[dl], v57\n" \
  "v_mfma_f32_16x16x32_bf16 v[104:107], v[216:219], v[232:235], v[104:107]\n" \
  "v_add_u32 v58, %[dl], v58\n" \
  "v_mfma_f32_16x16x32_bf16 v[108:111], v[220:223], v[232:235], v[108:111]\n" \
  "v_add_u32 v59, %[dl], v59\n" \
  "s_waitcnt lgkmcnt(4)\n" \
  "v_mfma_f32_16x16x32_bf16 v[124:127], v[220:223], v[236:239], v[124:127]\n" \
  "v_mfma_f32_16x16x32_bf16 v[120:123], v[216:219], v[236:239], v[120:123]\n" \
  "s_sub_u32 %[cnt], %[cnt], 1\n" \
  "v_mfma_f32_16x16x32_bf16 v[116:119], v[212:215], v[236:239], v[116:119]\n" \
  "v_mfma_f32_16x16x32_bf16 v[112:115], v[208:211], v[236:239], v[112:115]\n" \
  "s_waitcnt lgkmcnt(0)\n" \
  "s_barrier\n" \
  "v_mfma_f32_16x16x32_bf16 v[128:131], v[208:211], v[240:243], v[128:131]\n" \
  "ds_read_b64_tr_b16 v[192:193], v48 offset:0\n" \
  "v_mfma_f32_16x16x32_bf16 v[132:135], v[212:215], v[240:243], v[132:135]\n" \
  "ds_read_b64_tr_b16 v[194:195], v49 offset:0\n" \
  "v_mfma_f32_16x16x32_bf16 v[136:139], v[216:219], v[240:243], v[136:139]\n" \
  "ds_read_b128 v[224:227], v56 offset:0\n" \
  "v_mfma_f32_16x16x32_bf16 v[140:143], v[220:223], v[240:243], v[140:143]\n" \
  "ds_read_b64_tr_b16 v[196:197], v50 offset:0\n" \
  "v_mfma_f32_16x16x32_bf16 v[156:159], v[220:223], v[244:247], v[156:159]\n" \
  "ds_read_b64_tr_b16 v[198:199], v51 offset:0\n" \
  "v_mfma_f32_16x16x32_bf16 v[152:155], v[216:219], v[244:247], v[152:155]\n" \
  "ds_read_b64_tr_b16 v[200:201], v52 offset:0\n" \
  "v_mfma_f32_16x16x32_bf16 v[148:151], v[212:215], v[244:247], v[148:151]\n" \
  "ds_read_b64_tr_b16 v[202:203], v53 offset:0\n" \
  "v_mfma_f32_16x16x32_bf16 v[144:147], v[208:211], v[244:247], v[144:147]\n" \
  "ds_read_b64_tr_b16 v[204:205], v54 offset:0\n" \
  "v_mfma_f32_16x16x32_bf16 v[160:163], v[208:211], v[248:251], v[160:163]\n" \
  "ds_read_b64_tr_b16 v[206:207], v55 offset:0\n" \
  "v_mfma_f32_16x16x32_bf16 v[164:167], v[212:215], v[248:251], v[164:167]\n" \
  "ds_read_b128 v[228:231], v56 offset:2048\n" \
  "v_mfma_f32_16x16x32_bf16 v[168:171], v[216:219], v[248:251], v[168:171]\n" \
  "ds_read_b128 v[232:235], v56 offset:4096\n" \
  "v_mfma_f32_16x16x32_bf16 v[172:175], v[220:223], v[248:251], v[172:175]\n" \
  "ds_read_b128 v[236:239], v56 offset:6144\n" \
  "v_mfma_f32_16x16x32_bf16 v[188:191], v[220:223], v[252:255], v[188:191]\n" \
  "ds_read_b128 v[240:243], v57 offset:0\n" \
  "v_mfma_f32_16x16x32_bf16 v[184:187], v[216:219], v[252:255], v[184:187]\n" \
  "ds_read_b128 v[244:247], v57 offset:2048\n" \
  "v_mfma_f32_16x16x32_bf16 v[180:183], v[212:215], v[252:255], v[180:183]\n" \
  "s_cmp_lg_u32 %[cnt], 0\n" \
  "v_mfma_f32_16x16x32_bf16 v[176:179], v[208:211], v[252:255], v[176:179]\n" \
  "s_cbranch_scc1 1b\n" \
  "ds_read_b128 v[248:251], v57 offset:4096\n" \
  "ds_read_b128 v[252:255], v57 offset:6144\n" \
  "s_waitcnt lgkmcnt(3)\n" \
  "v_mfma_f32_16x16x32_bf16 v[128:131], v[192:195], v[240:243], v[128:131]\n" \
  "ds_read_b128 v[224:227], v59 offset:0\n" \
  "v_mfma_f32_16x16x32_bf16 v[132:135], v[196:199], v[240:243], v[132:135]\n" \
  "ds_read_b64_tr_b16 v[208:209], v48 offset:8192\n" \
  "v_mfma_f32_16x16x32_bf16 v[136:139], v[200:203], v[240:243], v[136:139]\n" \
  "ds_read_b64_tr_b16 v[210:211], v49 offset:8192\n" \
  "v_mfma_f32_16x16x32_bf16 v[140:143], v[204:207], v[240:243], v[140:143]\n" \
  "ds_read_b64_tr_b16 v[212:213], v50 offset:8192\n" \
  "s_waitcnt lgkmcnt(6)\n" \
  "v_mfma_f32_16x16x32_bf16 v[156:159], v[204:207], v[244:247], v[156:159]\n" \
  "ds_read_b64_tr_b16 v[214:215], v51 offset:8192\n" \
  "v_mfma_f32_16x16x32_bf16 v[152:155], v[200:203], v[244:247], v[152:155]\n" \
  "ds_read_b128 v[228:231], v59 offset:2048\n" \
  "v_mfma_f32_16x16x32_bf16 v[148:151], v[196:199], v[244:247], v[148:151]\n" \
  "ds_read_b64_tr_b16 v[216:217], v52 offset:8192\n" \
  "v_mfma_f32_16x16x32_bf16 v[144:147], v[192:195], v[244:247], v[144:147]\n" \
  "ds_read_b64_tr_b16 v[218:219], v53 offset:8192\n" \
  "s_waitcnt lgkmcnt(9)\n" \
  "v_mfma_f32_16x16x32_bf16 v[160:163], v[192:195], v[248:251], v[160:163]\n" \
  "ds_read_b64_tr_b16 v[220:221], v54 offset:8192\n" \
  "v_mfma_f32_16x16x32_bf16 v[164:167], v[196:199], v[248:251], v[164:167]\n" \
  "ds_read_b64_tr_b16 v[222:223], v55 offset:8192\n" \
  "v_mfma_f32_16x16x32_bf16 v[168:171], v[200:203], v[248:251], v[168:171]\n" \
  "ds_read_b128 v[232:235], v59 offset:4096\n" \
  "v_mfma_f32_16x16x32_bf16 v[172:175], v[204:207], v[248:251], v[172:175]\n" \
  "ds_read_b128 v[236:239], v59 offset:6144\n" \
  "s_waitcnt lgkmcnt(12)\n" \
  "v_mfma_f32_16x16x32_bf16 v[188:191], v[204:207], v[252:255], v[188:191]\n" \
  "v_mfma_f32_16x16x32_bf16 v[184:187], v[200:203], v[252:255], v[184:187]\n" \
  "v_mfma_f32_16x16x32_bf16 v[180:183], v[196:199], v[252:255], v[180:183]\n" \
  "v_mfma_f32_16x16x32_bf16 v[176:179], v[192:195], v[252:255], v[176:179]\n" \
  "s_waitcnt lgkmcnt(9)\n" \
  "v_mfma_f32_16x16x32_bf16 v[128:131], v[208:211], v[224:227], v[128:131]\n" \
  "ds_read_b128 v[240:243], v58 offset:0\n" \
  "s_waitcnt lgkmcnt(8)\n" \
  "v_mfma_f32_16x16x32_bf16 v[132:135], v[212:215], v[224:227], v[132:135]\n" \
  "ds_read_b128 v[244:247], v58 offset:2048\n" \
  "s_waitcnt lgkmcnt(6)\n" \
  "v_mfma_f32_16x16x32_bf16 v[136:139], v[216:219], v[224:227], v[136:139]\n" \
  "ds_read_b128 v[248:251], v58 offset:4096\n" \
  "s_waitcnt lgkmcnt(5)\n" \
  "v_mfma_f32_16x16x32_bf16 v[140:143], v[220:223], v[224:227], v[140:143]\n" \
  "ds_read_b128 v[252:255], v58 offset:6144\n" \
  "v_mfma_f32_16x16x32_bf16 v[156:159], v[220:223], v[228:231], v[156:159]\n" \
  "v_mfma_f32_16x16x32_bf16 v[152:155], v[216:219], v[228:231], v[152:155]\n" \
  "v_mfma_f32_16x16x32_bf16 v[148:151], v[212:215], v[228:231], v[148:151]\n" \
  "v_mfma_f32_16x16x32_bf16 v[144:147], v[208:211], v[228:231], v[144:147]\n" \
  "s_waitcnt lgkmcnt(5)\n" \
  "v_mfma_f32_16x16x32_bf16 v[160:163], v[208:211], v[232:235], v[160:163]\n" \
  "v_mfma_f32_16x16x32_bf16 v[164:167], v[212:215], v[232:235], v[164:167]\n" \
  "v_mfma_f32_16x16x32_bf16 v[168:171], v[216:219], v[232:235], v[168:171]\n" \
  "v_mfma_f32_16x16x32_bf16 v[172:175], v[220:223], v[232:235], v[172:175]\n" \
  "s_waitcnt lgkmcnt(4)\n" \
  "v_mfma_f32_16x16x32_bf16 v[188:191], v[220:223], v[236:239], v[188:191]\n" \
  "v_mfma_f32_16x16x32_bf16 v[184:187], v[216:219], v[236:239], v[184:187]\n" \
  "v_mfma_f32_16x16x32_bf16 v[180:183], v[212:215], v[236:239], v[180:183]\n" \
  "v_mfma_f32_16x16x32_bf16 v[176:179], v[208:211], v[236:239], v[176:179]\n" \
  "s_nop 3\n" \
  "v_cvt_pk_bf16_f32 v128, v128, v129\n" \
  "v_cvt_pk_bf16_f32 v129, v130, v131\n" \
  "v_cvt_pk_bf16_f32 v130, v132, v133\n" \
  "v_cvt_pk_bf16_f32 v131, v134, v135\n" \
  "v_min_u32 v128, -2, v128\n" \
  "v_min_u32 v129, -2, v129\n" \
  "v_min_u32 v130, -2, v130\n" \
  "v_min_u32 v131, -2, v131\n" \
  "buffer_store_dwordx4 v[128:131], v40, %[rsx], %[spr] offen offset:0 sc1\n" \
  "v_cvt_pk_bf16_f32 v136, v136, v137\n" \
  "v_cvt_pk_bf16_f32 v137, v138, v139\n" \
  "v_cvt_pk_bf16_f32 v138, v140, v141\n" \
  "v_cvt_pk_bf16_f32 v139, v142, v143\n" \
  "v_min_u32 v136, -2, v136\n" \
  "v_min_u32 v137, -2, v137\n" \
  "v_min_u32 v138, -2, v138\n" \
  "v_min_u32 v139, -2, v139\n" \
  "buffer_store_dwordx4 v[136:139], v40, %[rsx], %[spr] offen offset:1024 sc1\n" \
  "v_cvt_pk_bf16_f32 v144, v144, v145\n" \
  "v_cvt_pk_bf16_f32 v145, v146, v147\n" \
  "v_cvt_pk_bf16_f32 v146, v148, v149\n" \
  "v_cvt_pk_bf16_f32 v147, v150, v151\n" \
  "v_min_u32 v144, -2, v144\n" \
  "v_min_u32 v145, -2, v145\n" \
  "v_min_u32 v146, -2, v146\n" \
  "v_min_u32 v147, -2, v147\n" \
  "buffer_store_dwordx4 v[144:147], v40, %[rsx], %[spr] offen offset:2048 sc1\n" \
  "v_cvt_pk_bf16_f32 v152, v152, v153\n" \
  "v_cvt_pk_bf16_f32 v153, v154, v155\n" \
  "v_cvt_pk_bf16_f32 v154, v156, v157\n" \
  "v_cvt_pk_bf16_f32 v155, v158, v159\n" \
  "v_min_u32 v152, -2, v152\n" \
  "v_min_u32 v153, -2, v153\n" \
  "v_min_u32 v154, -2, v154\n" \
  "v_min_u32 v155, -2, v155\n" \
  "buffer_store_dwordx4 v[152:155], v40, %[rsx], %[spr] offen offset:3072 sc1\n" \
  "v_cvt_pk_bf16_f32 v160, v160, v161\n" \
  "v_cvt_pk_bf16_f32 v161, v162, v163\n" \
  "v_cvt_pk_bf16_f32 v162, v164, v165\n" \
  "v_cvt_pk_bf16_f32 v163, v166, v167\n" \
  "v_min_u32 v160, -2, v160\n" \
  "v_min_u32 v161, -2, v161\n" \
  "v_min_u32 v162, -2, v162\n" \
  "v_min_u32 v163, -2, v163\n" \
  "buffer_store_dwordx4 v[160:163], v41, %[rsx], %[spr] offen offset:0 sc1\n" \
  "v_cvt_pk_bf16_f32 v168, v168, v169\n" \
  "v_cvt_pk_bf16_f32 v169, v170, v171\n" \
  "v_cvt_pk_bf16_f32 v170, v172, v173\n" \
  "v_cvt_pk_bf16_f32 v171, v174, v175\n" \
  "v_min_u32 v168, -2, v168\n" \
  "v_min_u32 v169, -2, v169\n" \
  "v_min_u32 v170, -2, v170\n" \
  "v_min_u32 v171, -2, v171\n" \
  "buffer_store_dwordx4 v[168:171], v41, %[rsx], %[spr] offen offset:1024 sc1\n" \
  "v_cvt_pk_bf16_f32 v176, v176, v177\n" \
  "v_cvt_pk_bf16_f32 v177, v178, v179\n" \
  "v_cvt_pk_bf16_f32 v178, v180, v181\n" \
  "v_cvt_pk_bf16_f32 v179, v182, v183\n" \
  "v_min_u32 v176, -2, v176\n" \
  "v_min_u32 v177, -2, v177\n" \
  "v_min_u32 v178, -2, v178\n" \
  "v_min_u32 v179, -2, v179\n" \
  "buffer_store_dwordx4 v[176:179], v41, %[rsx], %[spr] offen offset:2048 sc1\n" \
  "v_cvt_pk_bf16_f32 v184, v184, v185\n" \
  "v_cvt_pk_bf16_f32 v185, v186, v187\n" \
  "v_cvt_pk_bf16_f32 v186, v188, v189\n" \
  "v_cvt_pk_bf16_f32 v187, v190, v191\n" \
  "v_min_u32 v184, -2, v184\n" \
  "v_min_u32 v185, -2, v185\n" \
  "v_min_u32 v186, -2, v186\n" \
  "v_min_u32 v187, -2, v187\n" \
  "buffer_store_dwordx4 v[184:187], v41, %[rsx], %[spr] offen offset:3072 sc1\n" \
  "s_waitcnt lgkmcnt(3)\n" \
  "v_mfma_f32_16x16x32_bf16 v[64:67], v[208:211], v[240:243], v[64:67]\n" \
  "ds_read_b128 v[224:227], v56 offset:0\n" \
  "v_mfma_f32_16x16x32_bf16 v[68:71], v[212:215], v[240:243], v[68:71]\n" \
  "ds_read_b128 v[228:231], v56 offset:2048\n" \
  "v_mfma_f32_16x16x32_bf16 v[72:75], v[216:219], v[240:243], v[72:75]\n" \
  "ds_read_b128 v[232:235], v56 offset:4096\n" \
  "v_mfma_f32_16x16x32_bf16 v[76:79], v[220:223], v[240:243], v[76:79]\n" \
  "ds_read_b128 v[236:239], v56 offset:6144\n" \
  "s_waitcnt lgkmcnt(6)\n" \
  "v_mfma_f32_16x16x32_bf16 v[92:95], v[220:223], v[244:247], v[92:95]\n" \
  "v_mfma_f32_16x16x32_bf16 v[88:91], v[216:219], v[244:247], v[88:91]\n" \
  "v_mfma_f32_16x16x32_bf16 v[84:87], v[212:215], v[244:247], v[84:87]\n" \
  "v_add_u32 v48, 0xfffe4000, v48\n" \
  "v_mfma_f32_16x16x32_bf16 v[80:83], v[208:211], v[244:247], v[80:83]\n" \
  "v_add_u32 v49, 0xfffe4000, v49\n" \
  "s_waitcnt lgkmcnt(5)\n" \
  "v_mfma_f32_16x16x32_bf16 v[96:99], v[208:211], v[248:251], v[96:99]\n" \
  "v_add_u32 v50, 0xfffe4000, v50\n" \
  "v_mfma_f32_16x16x32_bf16 v[100:103], v[212:215], v[248:251], v[100:103]\n" \
  "v_add_u32 v51, 0xfffe4000, v51\n" \
  "v_mfma_f32_16x16x32_bf16 v[104:107], v[216:219], v[248:251], v[104:107]\n" \
  "v_add_u32 v52, 0xfffe4000, v52\n" \
  "v_mfma_f32_16x16x32_bf16 v[108:111], v[220:223], v[248:251], v[108:111]\n" \
  "v_add_u32 v53, 0xfffe4000, v53\n" \
  "s_waitcnt lgkmcnt(4)\n" \
  "v_mfma_f32_16x16x32_bf16 v[124:127], v[220:223], v[252:255], v[124:127]\n" \
  "v_add_u32 v54, 0xfffe4000, v54\n" \
  "v_mfma_f32_16x16x32_bf16 v[120:123], v[216:219], v[252:255], v[120:123]\n" \
  "v_add_u32 v55, 0xfffe4000, v55\n" \
  "v_mfma_f32_16x16x32_bf16 v[116:119], v[212:215], v[252:255], v[116:119]\n" \
  "v_mfma_f32_16x16x32_bf16 v[112:115], v[208:211], v[252:255], v[112:115]\n" \
  "s_waitcnt lgkmcnt(3)\n" \
  "v_mfma_f32_16x16x32_bf16 v[64:67], v[192:195], v[224:227], v[64:67]\n" \
  "v_add_u32 v56, %[dx], v56\n" \
  "v_add_u32 v58, %[dx], v58\n" \
  "v_mfma_f32_16x16x32_bf16 v[68:71], v[196:199], v[224:227], v[68:71]\n" \
  "v_mfma_f32_16x16x32_bf16 v[72:75], v[200:203], v[224:227], v[72:75]\n" \
  "v_mfma_f32_16x16x32_bf16 v[76:79], v[204:207], v[224:227], v[76:79]\n" \
  "s_waitcnt lgkmcnt(0)\n" \
  "s_barrier\n" \
  "ds_read_b128 v[240:243], v56 offset:0\n" \
  "v_mfma_f32_16x16x32_bf16 v[92:95], v[204:207], v[228:231], v[92:95]\n" \
  "ds_read_b64_tr_b16 v[208:209], v48 offset:0\n" \
  "v_mfma_f32_16x16x32_bf16 v[88:91], v[200:203], v[228:231], v[88:91]\n" \
  "ds_read_b64_tr_b16 v[210:211], v49 offset:0\n" \
  "v_mfma_f32_16x16x32_bf16 v[84:87], v[196:199], v[228:231], v[84:87]\n" \
  "ds_read_b64_tr_b16 v[212:213], v50 offset:0\n" \
  "v_mfma_f32_16x16x32_bf16 v[80:83], v[192:195], v[228:231], v[80:83]\n" \
  "ds_read_b64_tr_b16 v[214:215], v51 offset:0\n" \
  "v_mfma_f32_16x16x32_bf16 v[96:99], v[192:195], v[232:235], v[96:99]\n" \
  "ds_read_b128 v[244:247], v56 offset:2048\n" \
  "v_mfma_f32_16x16x32_bf16 v[100:103], v[196:199], v[232:235], v[100:103]\n" \
  "ds_read_b64_tr_b16 v[216:217], v52 offset:0\n" \
  "v_mfma_f32_16x16x32_bf16 v[104:107], v[200:203], v[232:235], v[104:107]\n" \
  "ds_read_b64_tr_b16 v[218:219], v53 offset:0\n" \
  "v_mfma_f32_16x16x32_bf16 v[108:111], v[204:207], v[232:235], v[108:111]\n" \
  "ds_read_b64_tr_b16 v[220:221], v54 offset:0\n" \
  "v_mfma_f32_16x16x32_bf16 v[124:127], v[204:207], v[236:239], v[124:127]\n" \
  "ds_read_b64_tr_b16 v[222:223], v55 offset:0\n" \
  "v_mfma_f32_16x16x32_bf16 v[120:123], v[200:203], v[236:239], v[120:123]\n" \
  "ds_read_b128 v[248:251], v56 offset:4096\n" \
  "v_mfma_f32_16x16x32_bf16 v[116:119], v[196:199], v[236:239], v[116:119]\n" \
  "ds_read_b128 v[252:255], v56 offset:6144\n" \
  "v_mfma_f32_16x16x32_bf16 v[112:115], v[192:195], v[236:239], v[112:115]\n" \
  "s_waitcnt lgkmcnt(9)\n" \
  "v_mfma_f32_16x16x32_bf16 v[64:67], v[208:211], v[240:243], v[64:67]\n" \
  "ds_read_b128 v[224:227], v58 offset:0\n" \
  "s_waitcnt lgkmcnt(8)\n" \
  "v_mfma_f32_16x16x32_bf16 v[68:71], v[212:215], v[240:243], v[68:71]\n" \
  "ds_read_b64_tr_b16 v[192:193], v48 offset:8192\n" \
  "s_waitcnt lgkmcnt(6)\n" \
  "v_mfma_f32_16x16x32_bf16 v[72:75], v[216:219], v[240:243], v[72:75]\n" \
  "ds_read_b64_tr_b16 v[194:195], v49 offset:8192\n" \
  "s_waitcnt lgkmcnt(5)\n" \
  "v_mfma_f32_16x16x32_bf16 v[76:79], v[220:223], v[240:243], v[76:79]\n" \
  "ds_read_b64_tr_b16 v[196:197], v50 offset:8192\n" \
  "v_mfma_f32_16x16x32_bf16 v[92:95], v[220:223], v[244:247], v[92:95]\n" \
  "ds_read_b64_tr_b16 v[198:199], v51 offset:8192\n" \
  "v_mfma_f32_16x16x32_bf16 v[88:91], v[216:219], v[244:247], v[88:91]\n" \
  "ds_read_b128 v[228:231], v58 offset:2048\n" \
  "v_mfma_f32_16x16x32_bf16 v[84:87], v[212:215], v[244:247], v[84:87]\n" \
  "ds_read_b64_tr_b16 v[200:201], v52 offset:8192\n" \
  "v_mfma_f32_16x16x32_bf16 v[80:83], v[208:211], v[244:247], v[80:83]\n" \
  "ds_read_b64_tr_b16 v[202:203], v53 offset:8192\n" \
  "s_waitcnt lgkmcnt(9)\n" \
  "v_mfma_f32_16x16x32_bf16 v[96:99], v[208:211], v[248:251], v[96:99]\n" \
  "ds_read_b64_tr_b16 v[204:205], v54 offset:8192\n" \
  "v_mfma_f32_16x16x32_bf16 v[100:103], v[212:215], v[248:251], v[100:103]\n" \
  "ds_read_b64_tr_b16 v[206:207], v55 offset:8192\n" \
  "v_mfma_f32_16x16x32_bf16 v[104:107], v[216:219], v[248:251], v[104:107]\n" \
  "ds_read_b128 v[232:235], v58 offset:4096\n" \
  "v_mfma_f32_16x16x32_bf16 v[108:111], v[220:223], v[248:251], v[108:111]\n" \
  "ds_read_b128 v[236:239], v58 offset:6144\n" \
  "s_waitcnt lgkmcnt(12)\n" \
  "v_mfma_f32_16x16x32_bf16 v[124:127], v[220:223], v[252:255], v[124:127]\n" \
  "v_mfma_f32_16x16x32_bf16 v[120:123], v[216:219], v[252:255], v[120:123]\n" \
  "v_mfma_f32_16x16x32_bf16 v[116:119], v[212:215], v[252:255], v[116:119]\n" \
  "v_mfma_f32_16x16x32_bf16 v[112:115], v[208:211], v[252:255], v[112:115]\n" \
  "s_waitcnt lgkmcnt(9)\n" \
  "v_mfma_f32_16x16x32_bf16 v[64:67], v[192:195], v[224:227], v[64:67]\n" \
  "ds_read_b128 v[240:243], v56 offset:32768\n" \
  "s_waitcnt lgkmcnt(8)\n" \
  "v_mfma_f32_16x16x32_bf16 v[68:71], v[196:199], v[224:227], v[68:71]\n" \
  "ds_read_b64_tr_b16 v[208:209], v48 offset:32768\n" \
  "s_waitcnt lgkmcnt(6)\n" \
  "v_mfma_f32_16x16x32_bf16 v[72:75], v[200:203], v[224:227], v[72:75]\n" \
  "ds_read_b64_tr_b16 v[210:211], v49 offset:32768\n" \
  "s_waitcnt lgkmcnt(5)\n" \
  "v_mfma_f32_16x16x32_bf16 v[76:79], v[204:207], v[224:227], v[76:79]\n" \
  "ds_read_b64_tr_b16 v[212:213], v50 offset:32768\n" \
  "v_mfma_f32_16x16x32_bf16 v[92:95], v[204:207], v[228:231], v[92:95]\n" \
  "ds_read_b64_tr_b16 v[214:215], v51 offset:32768\n" \
  "v_mfma_f32_16x16x32_bf16 v[88:91], v[200:203], v[228:231], v[88:91]\n" \
  "ds_read_b128 v[244:247], v56 offset:34816\n" \
  "v_mfma_f32_16x16x32_bf16 v[84:87], v[196:199], v[228:231], v[84:87]\n" \
  "ds_read_b64_tr_b16 v[216:217], v52 offset:32768\n" \
  "v_mfma_f32_16x16x32_bf16 v[80:83], v[192:195], v[228:231], v[80:83]\n" \
  "ds_read_b64_tr_b16 v[218:219], v53 offset:32768\n" \
  "s_waitcnt lgkmcnt(9)\n" \
  "v_mfma_f32_16x16x32_bf16 v[96:99], v[192:195], v[232:235], v[96:99]\n" \
  "ds_read_b64_tr_b16 v[220:221], v54 offset:32768\n" \
  "v_mfma_f32_16x16x32_bf16 v[100:103], v[196:199], v[232:235], v[100:103]\n" \
  "ds_read_b64_tr_b16 v[222:223], v55 offset:32768\n" \
  "v_mfma_f32_16x16x32_bf16 v[104:107], v[200:203], v[232:235], v[104:107]\n" \
  "ds_read_b128 v[248:251], v56 offset:36864\n" \
  "v_mfma_f32_16x16x32_bf16 v[108:111], v[204:207], v[232:235], v[108:111]\n" \
  "ds_read_b128 v[252:255], v56 offset:38912\n" \
  "s_waitcnt lgkmcnt(12)\n" \
  "v_mfma_f32_16x16x32_bf16 v[124:127], v[204:207], v[236:239], v[124:127]\n" \
  "v_mfma_f32_16x16x32_bf16 v[120:123], v[200:203], v[236:239], v[120:123]\n" \
  "v_mfma_f32_16x16x32_bf16 v[116:119], v[196:199], v[236:239], v[116:119]\n" \
  "v_mfma_f32_16x16x32_bf16 v[112:115], v[192:195], v[236:239], v[112:115]\n" \
  "s_waitcnt lgkmcnt(9)\n" \
  "v_mfma_f32_16x16x32_bf16 v[64:67], v[208:211], v[240:243], v[64:67]\n" \
  "ds_read_b128 v[224:227], v58 offset:32768\n" \
  "s_waitcnt lgkmcnt(8)\n" \
  "v_mfma_f32_16x16x32_bf16 v[68:71], v[212:215], v[240:243], v[68:71]\n" \
  "ds_read_b64_tr_b16 v[192:193], v48 offset:40960\n" \
  "s_waitcnt lgkmcnt(6)\n" \
  "v_mfma_f32_16x16x32_bf16 v[72:75], v[216:219], v[240:243], v[72:75]\n" \
  "ds_read_b64_tr_b16 v[194:195], v49 offset:40960\n" \
  "s_waitcnt lgkmcnt(5)\n" \
  "v_mfma_f32_16x16x32_bf16 v[76:79], v[220:223], v[240:243], v[76:79]\n" \
  "ds_read_b64_tr_b16 v[196:197], v50 offset:40960\n" \
  "v_mfma_f32_16x16x32_bf16 v[92:95], v[220:223], v[244:247], v[92:95]\n" \
  "ds_read_b64_tr_b16 v[198:199], v51 offset:40960\n" \
  "v_mfma_f32_16x16x32_bf16 v[88:91], v[216:219], v[244:247], v[88:91]\n" \
  "ds_read_b128 v[228:231], v58 offset:34816\n" \
  "v_mfma_f32_16x16x32_bf16 v[84:87], v[212:215], v[244:247], v[84:87]\n" \
  "ds_read_b64_tr_b16 v[200:201], v52 offset:40960\n" \
  "v_mfma_f32_16x16x32_bf16 v[80:83], v[208:211], v[244:247], v[80:83]\n" \
  "ds_read_b64_tr_b16 v[202:203], v53 offset:40960\n" \
  "s_waitcnt lgkmcnt(9)\n" \
  "v_mfma_f32_16x16x32_bf16 v[96:99], v[208:211], v[248:251], v[96:99]\n" \
  "ds_read_b64_tr_b16 v[204:205], v54 offset:40960\n" \
  "v_mfma_f32_16x16x32_bf16 v[100:103], v[212:215], v[248:251], v[100:103]\n" \
  "ds_read_b64_tr_b16 v[206:207], v55 offset:40960\n" \
  "v_mfma_f32_16x16x32_bf16 v[104:107], v[216:219], v[248:251], v[104:107]\n" \
  "ds_read_b128 v[232:235], v58 offset:36864\n" \
  "v_mfma_f32_16x16x32_bf16 v[108:111], v[220:223], v[248:251], v[108:111]\n" \
  "ds_read_b128 v[236:239], v58 offset:38912\n" \
  "s_waitcnt lgkmcnt(12)\n" \
  "v_mfma_f32_16x16x32_bf16 v[124:127], v[220:223], v[252:255], v[124:127]\n" \
  "v_mfma_f32_16x16x32_bf16 v[120:123], v[216:219], v[252:255], v[120:123]\n" \
  "v_mfma_f32_16x16x32_bf16 v[116:119], v[212:215], v[252:255], v[116:119]\n" \
  "v_mfma_f32_16x16x32_bf16 v[112:115], v[208:211], v[252:255], v[112:115]\n" \
  "s_setprio 3\n" \
  "s_waitcnt lgkmcnt(9)\n" \
  "v_mfma_f32_16x16x32_bf16 v[64:67], v[192:195], v[224:227], v[64:67]\n" \
  "v_add_u32 v48, 0x10000, v48\n" \
  "v_add_u32 v49, 0x10000, v49\n" \
  "v_add_u32 v50, 0x10000, v50\n" \
  "s_waitcnt lgkmcnt(7)\n" \
  "v_mfma_f32_16x16x32_bf16 v[68:71], v[196:199], v[224:227], v[68:71]\n" \
  "v_add_u32 v51, 0x10000, v51\n" \
  "v_add_u32 v52, 0x10000, v52\n" \
  "s_waitcnt lgkmcnt(4)\n" \
  "v_mfma_f32_16x16x32_bf16 v[72:75], v[200:203], v[224:227], v[72:75]\n" \
  "v_add_u32 v53, 0x10000, v53\n" \
  "v_add_u32 v54, 0x10000, v54\n" \
  "v_add_u32 v55, 0x10000, v55\n" \
  "s_waitcnt lgkmcnt(2)\n" \
  "v_mfma_f32_16x16x32_bf16 v[76:79], v[204:207], v[224:227], v[76:79]\n" \
  "v_add_u32 v56, 0x10000, v56\n" \
  "v_add_u32 v58, 0x10000, v58\n" \
  "s_waitcnt lgkmcnt(0)\n" \
  "s_barrier\n" \
  "ds_read_b128 v[240:243], v56 offset:0\n" \
  "v_mfma_f32_16x16x32_bf16 v[92:95], v[204:207], v[228:231], v[92:95]\n" \
  "ds_read_b64_tr_b16 v[208:209], v48 offset:0\n" \
  "v_mfma_f32_16x16x32_bf16 v[88:91], v[200:203], v[228:231], v[88:91]\n" \
  "ds_read_b64_tr_b16 v[210:211], v49 offset:0\n" \
  "v_mfma_f32_16x16x32_bf16 v[84:87], v[196:199], v[228:231], v[84:87]\n" \
  "ds_read_b64_tr_b16 v[212:213], v50 offset:0\n" \
  "v_mfma_f32_16x16x32_bf16 v[80:83], v[192:195], v[228:231], v[80:83]\n" \
  "ds_read_b64_tr_b16 v[214:215], v51 offset:0\n" \
  "v_mfma_f32_16x16x32_bf16 v[96:99], v[192:195], v[232:235], v[96:99]\n" \
  "ds_read_b128 v[244:247], v56 offset:2048\n" \
  "v_mfma_f32_16x16x32_bf16 v[100:103], v[196:199], v[232:235], v[100:103]\n" \
  "ds_read_b64_tr_b16 v[216:217], v52 offset:0\n" \
  "v_mfma_f32_16x16x32_bf16 v[104:107], v[200:203], v[232:235], v[104:107]\n" \
  "ds_read_b64_tr_b16 v[218:219], v53 offset:0\n" \
  "v_mfma_f32_16x16x32_bf16 v[108:111], v[204:207], v[232:235], v[108:111]\n" \
  "ds_read_b64_tr_b16 v[220:221], v54 offset:0\n" \
  "v_mfma_f32_16x16x32_bf16 v[124:127], v[204:207], v[236:239], v[124:127]\n" \
  "ds_read_b64_tr_b16 v[222:223], v55 offset:0\n" \
  "v_mfma_f32_16x16x32_bf16 v[120:123], v[200:203], v[236:239], v[120:123]\n" \
  "ds_read_b128 v[248:251], v56 offset:4096\n" \
  "v_mfma_f32_16x16x32_bf16 v[116:119], v[196:199], v[236:239], v[116:119]\n" \
  "ds_read_b128 v[252:255], v56 offset:6144\n" \
  "v_mfma_f32_16x16x32_bf16 v[112:115], v[192:195], v[236:239], v[112:115]\n" \
  "s_waitcnt lgkmcnt(9)\n" \
  "v_mfma_f32_16x16x32_bf16 v[64:67], v[208:211], v[240:243], v[64:67]\n" \
  "ds_read_b128 v[224:227], v58 offset:0\n" \
  "s_waitcnt lgkmcnt(8)\n" \
  "v_mfma_f32_16x16x32_bf16 v[68:71], v[212:215], v[240:243], v[68:71]\n" \
  "ds_read_b64_tr_b16 v[192:193], v48 offset:8192\n" \
  "s_waitcnt lgkmcnt(6)\n" \
  "v_mfma_f32_16x16x32_bf16 v[72:75], v[216:219], v[240:243], v[72:75]\n" \
  "ds_read_b64_tr_b16 v[194:195], v49 offset:8192\n" \
  "s_waitcnt lgkmcnt(5)\n" \
  "v_mfma_f32_16x16x32_bf16 v[76:79], v[220:223], v[240:243], v[76:79]\n" \
  "ds_read_b64_tr_b16 v[196:197], v50 offset:8192\n" \
  "v_mfma_f32_16x16x32_bf16 v[92:95], v[220:223], v[244:247], v[92:95]\n" \
  "ds_read_b64_tr_b16 v[198:199], v51 offset:8192\n" \
  "v_mfma_f32_16x16x32_bf16 v[88:91], v[216:219], v[244:247], v[88:91]\n" \
  "ds_read_b128 v[228:231], v58 offset:2048\n" \
  "v_mfma_f32_16x16x32_bf16 v[84:87], v[212:215], v[244:247], v[84:87]\n" \
  "ds_read_b64_tr_b16 v[200:201], v52 offset:8192\n" \
  "v_mfma_f32_16x16x32_bf16 v[80:83], v[208:211], v[244:247], v[80:83]\n" \
  "ds_read_b64_tr_b16 v[202:203], v53 offset:8192\n" \
  "s_waitcnt lgkmcnt(9)\n" \
  "v_mfma_f32_16x16x32_bf16 v[96:99], v[208:211], v[248:251], v[96:99]\n" \
  "ds_read_b64_tr_b16 v[204:205], v54 offset:8192\n" \
  "v_mfma_f32_16x16x32_bf16 v[100:103], v[212:215], v[248:251], v[100:103]\n" \
  "ds_read_b64_tr_b16 v[206:207], v55 offset:8192\n" \
  "v_mfma_f32_16x16x32_bf16 v[104:107], v[216:219], v[248:251], v[104:107]\n" \
  "ds_read_b128 v[232:235], v58 offset:4096\n" \
  "v_mfma_f32_16x16x32_bf16 v[108:111], v[220:223], v[248:251], v[108:111]\n" \
  "ds_read_b128 v[236:239], v58 offset:6144\n" \
  "s_waitcnt lgkmcnt(12)\n" \
  "v_mfma_f32_16x16x32_bf16 v[124:127], v[220:223], v[252:255], v[124:127]\n" \
  "v_mfma_f32_16x16x32_bf16 v[120:123], v[216:219], v[252:255], v[120:123]\n" \
  "v_mfma_f32_16x16x32_bf16 v[116:119], v[212:215], v[252:255], v[116:119]\n" \
  "v_mfma_f32_16x16x32_bf16 v[112:115], v[208:211], v[252:255], v[112:115]\n" \
  "s_waitcnt lgkmcnt(9)\n" \
  "v_mfma_f32_16x16x32_bf16 v[64:67], v[192:195], v[224:227], v[64:67]\n" \
  "ds_read_b128 v[240:243], v56 offset:32768\n" \
  "s_waitcnt lgkmcnt(8)\n" \
  "v_mfma_f32_16x16x32_bf16 v[68:71], v[196:199], v[224:227], v[68:71]\n" \
  "ds_read_b64_tr_b16 v[208:209], v48 offset:32768\n" \
  "s_waitcnt lgkmcnt(6)\n" \
  "v_mfma_f32_16x16x32_bf16 v[72:75], v[200:203], v[224:227], v[72:75]\n" \
  "ds_read_b64_tr_b16 v[210:211], v49 offset:32768\n" \
  "s_waitcnt lgkmcnt(5)\n" \
  "v_mfma_f32_16x16x32_bf16 v[76:79], v[204:207], v[224:227], v[76:79]\n" \
  "ds_read_b64_tr_b16 v[212:213], v50 offset:32768\n" \
  "v_mfma_f32_16x16x32_bf16 v[92:95], v[204:207], v[228:231], v[92:95]\n" \
  "ds_read_b64_tr_b16 v[214:215], v51 offset:32768\n" \
  "buffer_load_dwordx4 v[132:135], v40, %[rsx], %[sme] offen offset:0 sc1\n" \
  "v_mfma_f32_16x16x32_bf16 v[88:91], v[200:203], v[228:231], v[88:91]\n" \
  "ds_read_b128 v[244:247], v56 offset:34816\n" \
  "buffer_load_dwordx4 v[140:143], v40, %[rsx], %[sme] offen offset:1024 sc1\n" \
  "v_mfma_f32_16x16x32_bf16 v[84:87], v[196:199], v[228:231], v[84:87]\n" \
  "ds_read_b64_tr_b16 v[216:217], v52 offset:32768\n" \
  "buffer_load_dwordx4 v[148:151], v40, %[rsx], %[sme] offen offset:2048 sc1\n" \
  "v_mfma_f32_16x16x32_bf16 v[80:83], v[192:195], v[228:231], v[80:83]\n" \
  "ds_read_b64_tr_b16 v[218:219], v53 offset:32768\n" \
  "buffer_load_dwordx4 v[156:159], v40, %[rsx], %[sme] offen offset:3072 sc1\n" \
  "s_waitcnt lgkmcnt(9)\n" \
  "v_mfma_f32_16x16x32_bf16 v[96:99], v[192:195], v[232:235], v[96:99]\n" \
  "ds_read_b64_tr_b16 v[220:221], v54 offset:32768\n" \
  "buffer_load_dwordx4 v[164:167], v41, %[rsx], %[sme] offen offset:0 sc1\n" \
  "v_mfma_f32_16x16x32_bf16 v[100:103], v[196:199], v[232:235], v[100:103]\n" \
  "ds_read_b64_tr_b16 v[222:223], v55 offset:32768\n" \
  "buffer_load_dwordx4 v[172:175], v41, %[rsx], %[sme] offen offset:1024 sc1\n" \
  "v_mfma_f32_16x16x32_bf16 v[104:107], v[200:203], v[232:235], v[104:107]\n" \
  "ds_read_b128 v[248:251], v56 offset:36864\n" \
  "buffer_load_dwordx4 v[180:183], v41, %[rsx], %[sme] offen offset:2048 sc1\n" \
  "v_mfma_f32_16x16x32_bf16 v[108:111], v[204:207], v[232:235], v[108:111]\n" \
  "ds_read_b128 v[252:255], v56 offset:38912\n" \
  "buffer_load_dwordx4 v[188:191], v41, %[rsx], %[sme] offen offset:3072 sc1\n" \
  "s_waitcnt lgkmcnt(12)\n" \
  "v_mfma_f32_16x16x32_bf16 v[124:127], v[204:207], v[236:239], v[124:127]\n" \
  "v_mfma_f32_16x16x32_bf16 v[120:123], v[200:203], v[236:239], v[120:123]\n" \
  "v_mfma_f32_16x16x32_bf16 v[116:119], v[196:199], v[236:239], v[116:119]\n" \
  "v_mfma_f32_16x16x32_bf16 v[112:115], v[192:195], v[236:239], v[112:115]\n" \
  "s_waitcnt lgkmcnt(9)\n" \
  "v_mfma_f32_16x16x32_bf16 v[64:67], v[208:211], v[240:243], v[64:67]\n" \
  "ds_read_b128 v[224:227], v58 offset:32768\n" \
  "s_waitcnt lgkmcnt(8)\n" \
  "v_mfma_f32_16x16x32_bf16 v[68:71], v[212:215], v[240:243], v[68:71]\n" \
  "ds_read_b64_tr_b16 v[192:193], v48 offset:40960\n" \
  "s_waitcnt lgkmcnt(6)\n" \
  "v_mfma_f32_16x16x32_bf16 v[72:75], v[216:219], v[240:243], v[72:75]\n" \
  "ds_read_b64_tr_b16 v[194:195], v49 offset:40960\n" \
  "s_waitcnt lgkmcnt(5)\n" \
  "v_mfma_f32_16x16x32_bf16 v[76:79], v[220:223], v[240:243], v[76:79]\n" \
  "ds_read_b64_tr_b16 v[196:197], v50 offset:40960\n" \
  "v_mfma_f32_16x16x32_bf16 v[92:95], v[220:223], v[244:247], v[92:95]\n" \
  "ds_read_b64_tr_b16 v[198:199], v51 offset:40960\n" \
  "v_mfma_f32_16x16x32_bf16 v[88:91], v[216:219], v[244:247], v[88:91]\n" \
  "ds_read_b128 v[228:231], v58 offset:34816\n" \
  "v_mfma_f32_16x16x32_bf16 v[84:87], v[212:215], v[244:247], v[84:87]\n" \
  "ds_read_b64_tr_b16 v[200:201], v52 offset:40960\n" \
  "v_mfma_f32_16x16x32_bf16 v[80:83], v[208:211], v[244:247], v[80:83]\n" \
  "ds_read_b64_tr_b16 v[202:203], v53 offset:40960\n" \
  "s_waitcnt lgkmcnt(9)\n" \
  "v_mfma_f32_16x16x32_bf16 v[96:99], v[208:211], v[248:251], v[96:99]\n" \
  "ds_read_b64_tr_b16 v[204:205], v54 offset:40960\n" \
  "v_mfma_f32_16x16x32_bf16 v[100:103], v[212:215], v[248:251], v[100:103]\n" \
  "ds_read_b64_tr_b16 v[206:207], v55 offset:40960\n" \
  "v_mfma_f32_16x16x32_bf16 v[104:107], v[216:219], v[248:251], v[104:107]\n" \
  "ds_read_b128 v[232:235], v58 offset:36864\n" \
  "v_mfma_f32_16x16x32_bf16 v[108:111], v[220:223], v[248:251], v[108:111]\n" \
  "ds_read_b128 v[236:239], v58 offset:38912\n" \
  "s_waitcnt lgkmcnt(12)\n" \
  "v_mfma_f32_16x16x32_bf16 v[124:127], v[220:223], v[252:255], v[124:127]\n" \
  "v_mfma_f32_16x16x32_bf16 v[120:123], v[216:219], v[252:255], v[120:123]\n" \
  "v_mfma_f32_16x16x32_bf16 v[116:119], v[212:215], v[252:255], v[116:119]\n" \
  "v_mfma_f32_16x16x32_bf16 v[112:115], v[208:211], v[252:255], v[112:115]\n" \
  "s_waitcnt lgkmcnt(9)\n" \
  "v_mfma_f32_16x16x32_bf16 v[64:67], v[192:195], v[224:227], v[64:67]\n" \
  "s_waitcnt lgkmcnt(7)\n" \
  "v_mfma_f32_16x16x32_bf16 v[68:71], v[196:199], v[224:227], v[68:71]\n" \
  "s_waitcnt lgkmcnt(4)\n" \
  "v_mfma_f32_16x16x32_bf16 v[72:75], v[200:203], v[224:227], v[72:75]\n" \
  "s_waitcnt lgkmcnt(2)\n" \
  "v_mfma_f32_16x16x32_bf16 v[76:79], v[204:207], v[224:227], v[76:79]\n" \
  "v_mfma_f32_16x16x32_bf16 v[92:95], v[204:207], v[228:231], v[92:95]\n" \
  "v_mfma_f32_16x16x32_bf16 v[88:91], v[200:203], v[228:231], v[88:91]\n" \
  "v_mfma_f32_16x16x32_bf16 v[84:87], v[196:199], v[228:231], v[84:87]\n" \
  "v_mfma_f32_16x16x32_bf16 v[80:83], v[192:195], v[228:231], v[80:83]\n" \
  "s_waitcnt lgkmcnt(1)\n" \
  "v_mfma_f32_16x16x32_bf16 v[96:99], v[192:195], v[232:235], v[96:99]\n" \
  "v_mfma_f32_16x16x32_bf16 v[100:103], v[196:199], v[232:235], v[100:103]\n" \
  "v_mfma_f32_16x16x32_bf16 v[104:107], v[200:203], v[232:235], v[104:107]\n" \
  "v_mfma_f32_16x16x32_bf16 v[108:111], v[204:207], v[232:235], v[108:111]\n" \
  "s_waitcnt lgkmcnt(0)\n" \
  "v_mfma_f32_16x16x32_bf16 v[124:127], v[204:207], v[236:239], v[124:127]\n" \
  "v_mfma_f32_16x16x32_bf16 v[120:123], v[200:203], v[236:239], v[120:123]\n" \
  "v_mfma_f32_16x16x32_bf16 v[116:119], v[196:199], v[236:239], v[116:119]\n" \
  "v_mfma_f32_16x16x32_bf16 v[112:115], v[192:195], v[236:239], v[112:115]\n" \
  "s_movk_i32 %[cnt], 0x7fff\n" \
  "v_mov_b32 v200, 0xc0135761\n" \
  "s_waitcnt vmcnt(0)\n" \
  "20:\n" \
  "v_max3_u32 v192, v132, v133, v134\n" \
  "v_max3_u32 v193, v135, v140, v141\n" \
  "v_max3_u32 v192, v142, v143, v192\n" \
  "v_max3_u32 v193, v148, v149, v193\n" \
  "v_max3_u32 v192, v150, v151, v192\n" \
  "v_max3_u32 v193, v156, v157, v193\n" \
  "v_max3_u32 v192, v158, v159, v192\n" \
  "v_max3_u32 v193, v164, v165, v193\n" \
  "v_max3_u32 v192, v166, v167, v192\n" \
  "v_max3_u32 v193, v172, v173, v193\n" \
  "v_max3_u32 v192, v174, v175, v192\n" \
  "v_max3_u32 v193, v180, v181, v193\n" \
  "v_max3_u32 v192, v182, v183, v192\n" \
  "v_max3_u32 v193, v188, v189, v193\n" \
  "v_max3_u32 v192, v190, v191, v192\n" \
  "v_max_u32 v192, v192, v193\n" \
  "s_sub_u32 %[cnt], %[cnt], 1\n" \
  "v_cmp_eq_u32 vcc, -1, v192\n" \
  "s_cbranch_scc1 22f\n" \
  "s_cbranch_vccz 21f\n" \
  "buffer_load_dwordx4 v[132:135], v40, %[rsx], %[sme] offen offset:0 sc1\n" \
  "buffer_load_dwordx4 v[140:143], v40, %[rsx], %[sme] offen offset:1024 sc1\n" \
  "buffer_load_dwordx4 v[148:151], v40, %[rsx], %[sme] offen offset:2048 sc1\n" \
  "buffer_load_dwordx4 v[156:159], v40, %[rsx], %[sme] offen offset:3072 sc1\n" \
  "buffer_load_dwordx4 v[164:167], v41, %[rsx], %[sme] offen offset:0 sc1\n" \
  "buffer_load_dwordx4 v[172:175], v41, %[rsx], %[sme] offen offset:1024 sc1\n" \
  "buffer_load_dwordx4 v[180:183], v41, %[rsx], %[sme] offen offset:2048 sc1\n" \
  "buffer_load_dwordx4 v[188:191], v41, %[rsx], %[sme] offen offset:3072 sc1\n" \
  "s_waitcnt vmcnt(0)\n" \
  "s_branch 20b\n" \
  "22:\n" \
  "v_mov_b32 v200, 0x7fc00000\n" \
  "21:\n" \
  "v_mfma_f32_16x16x32_bf16 v[64:67], v[44:47], v[132:135], v[64:67]\n" \
  "v_mfma_f32_16x16x32_bf16 v[68:71], v[42:45], v[132:135], v[68:71]\n" \
  "v_mfma_f32_16x16x32_bf16 v[72:75], v[44:47], v[140:143], v[72:75]\n" \
  "v_mfma_f32_16x16x32_bf16 v[76:79], v[42:45], v[140:143], v[76:79]\n" \
  "s_nop 4\n" \
  "s_nop 0\n" \
  "v_mul_f32 v204, v64, v64\n" \
  "v_mul_f32 v205, v65, v65\n" \
  "v_mul_f32 v206, v66, v66\n" \
  "v_mul_f32 v207, v67, v67\n" \
  "v_mul_f32 v208, v68, v68\n" \
  "v_mul_f32 v209, v69, v69\n" \
  "v_mul_f32 v210, v70, v70\n" \
  "v_mul_f32 v211, v71, v71\n" \
  "v_fmamk_f32 v204, v204, 0xbdd2d3e8, v200\n" \
  "v_fmamk_f32 v205, v205, 0xbdd2d3e8, v200\n" \
  "v_fmamk_f32 v206, v206, 0xbdd2d3e8, v200\n" \
  "v_fmamk_f32 v207, v207, 0xbdd2d3e8, v200\n" \
  "v_fmamk_f32 v208, v208, 0xbdd2d3e8, v200\n" \
  "v_fmamk_f32 v209, v209, 0xbdd2d3e8, v200\n" \
  "v_fmamk_f32 v210, v210, 0xbdd2d3e8, v200\n" \
  "v_fmamk_f32 v211, v211, 0xbdd2d3e8, v200\n" \
  "v_mfma_f32_16x16x32_bf16 v[80:83], v[44:47], v[148:151], v[80:83]\n" \
  "v_mfma_f32_16x16x32_bf16 v[84:87], v[42:45], v[148:151], v[84:87]\n" \
  "v_mul_f32 v204, v204, v64\n" \
  "v_mul_f32 v205, v205, v65\n" \
  "v_mul_f32 v206, v206, v66\n" \
  "v_mul_f32 v207, v207, v67\n" \
  "v_mul_f32 v208, v208, v68\n" \
  "v_mul_f32 v209, v209, v69\n" \
  "v_mul_f32 v210, v210, v70\n" \
  "v_mul_f32 v211, v211, v71\n" \
  "v_exp_f32 v204, v204\n" \
  "v_exp_f32 v205, v205\n" \
  "v_exp_f32 v206, v206\n" \
  "v_exp_f32 v207, v207\n" \
  "v_exp_f32 v208, v208\n" \
  "v_exp_f32 v209, v209\n" \
  "v_exp_f32 v210, v210\n" \
  "v_exp_f32 v211, v211\n" \
  "v_add_f32 v204, 1.0, v204\n" \
  "v_add_f32 v205, 1.0, v205\n" \
  "v_add_f32 v206, 1.0, v206\n" \
  "v_add_f32 v207, 1.0, v207\n" \
  "v_add_f32 v208, 1.0, v208\n" \
  "v_add_f32 v209, 1.0, v209\n" \
  "v_add_f32 v210, 1.0, v210\n" \
  "v_add_f32 v211, 1.0, v211\n" \
  "v_rcp_f32 v204, v204\n" \
  "v_rcp_f32 v205, v205\n" \
  "v_rcp_f32 v206, v206\n" \
  "v_rcp_f32 v207, v207\n" \
  "v_rcp_f32 v208, v208\n" \
  "v_rcp_f32 v209, v209\n" \
  "v_rcp_f32 v210, v210\n" \
  "v_rcp_f32 v211, v211\n" \
  "v_mul_f32 v204, v204, v64\n" \
  "v_mul_f32 v205, v205, v65\n" \
  "v_mul_f32 v206, v206, v66\n" \
  "v_mul_f32 v207, v207, v67\n" \
  "v_mul_f32 v208, v208, v68\n" \
  "v_mul_f32 v209, v209, v69\n" \
  "v_mul_f32 v210, v210, v70\n" \
  "v_mul_f32 v211, v211, v71\n" \
  "v_cvt_pk_bf16_f32 v212, v204, v205\n" \
  "v_cvt_pk_bf16_f32 v213, v206, v207\n" \
  "v_cvt_pk_bf16_f32 v214, v208, v209\n" \
  "v_cvt_pk_bf16_f32 v215, v210, v211\n" \
  "v_mul_f32 v204, v72, v72\n" \
  "v_mul_f32 v205, v73, v73\n" \
  "v_mul_f32 v206, v74, v74\n" \
  "v_mul_f32 v207, v75, v75\n" \
  "v_mul_f32 v208, v76, v76\n" \
  "v_mul_f32 v209, v77, v77\n" \
  "v_mul_f32 v210, v78, v78\n" \
  "v_mul_f32 v211, v79, v79\n" \
  "v_mfma_f32_16x16x32_bf16 v[224:227], v[32:35], v[212:215], 0\n" \
  "v_fmamk_f32 v204, v204, 0xbdd2d3e8, v200\n" \
  "v_fmamk_f32 v205, v205, 0xbdd2d3e8, v200\n" \
  "v_fmamk_f32 v206, v206, 0xbdd2d3e8, v200\n" \
  "v_fmamk_f32 v207, v207, 0xbdd2d3e8, v200\n" \
  "v_fmamk_f32 v208, v208, 0xbdd2d3e8, v200\n" \
  "v_fmamk_f32 v209, v209, 0xbdd2d3e8, v200\n" \
  "v_fmamk_f32 v210, v210, 0xbdd2d3e8, v200\n" \
  "v_fmamk_f32 v211, v211, 0xbdd2d3e8, v200\n" \
  "v_mfma_f32_16x16x32_bf16 v[88:91], v[44:47], v[156:159], v[88:91]\n" \
  "v_mfma_f32_16x16x32_bf16 v[92:95], v[42:45], v[156:159], v[92:95]\n" \
  "v_mul_f32 v204, v204, v72\n" \
  "v_mul_f32 v205, v205, v73\n" \
  "v_mul_f32 v206, v206, v74\n" \
  "v_mul_f32 v207, v207, v75\n" \
  "v_mul_f32 v208, v208, v76\n" \
  "v_mul_f32 v209, v209, v77\n" \
  "v_mul_f32 v210, v210, v78\n" \
  "v_mul_f32 v211, v211, v79\n" \
  "v_exp_f32 v204, v204\n" \
  "v_exp_f32 v205, v205\n" \
  "v_exp_f32 v206, v206\n" \
  "v_exp_f32 v207, v207\n" \
  "v_exp_f32 v208, v208\n" \
  "v_exp_f32 v209, v209\n" \
  "v_exp_f32 v210, v210\n" \
  "v_exp_f32 v211, v211\n" \
  "v_add_f32 v204, 1.0, v204\n" \
  "v_add_f32 v205, 1.0, v205\n" \
  "v_add_f32 v206, 1.0, v206\n" \
  "v_add_f32 v207, 1.0, v207\n" \
  "v_add_f32 v208, 1.0, v208\n" \
  "v_add_f32 v209, 1.0, v209\n" \
  "v_add_f32 v210, 1.0, v210\n" \
  "v_add_f32 v211, 1.0, v211\n" \
  "v_rcp_f32 v204, v204\n" \
  "v_rcp_f32 v205, v205\n" \
  "v_rcp_f32 v206, v206\n" \
  "v_rcp_f32 v207, v207\n" \
  "v_rcp_f32 v208, v208\n" \
  "v_rcp_f32 v209, v209\n" \
  "v_rcp_f32 v210, v210\n" \
  "v_rcp_f32 v211, v211\n" \
  "v_mul_f32 v204, v204, v72\n" \
  "v_mul_f32 v205, v205, v73\n" \
  "v_mul_f32 v206, v206, v74\n" \
  "v_mul_f32 v207, v207, v75\n" \
  "v_mul_f32 v208, v208, v76\n" \
  "v_mul_f32 v209, v209, v77\n" \
  "v_mul_f32 v210, v210, v78\n" \
  "v_mul_f32 v211, v211, v79\n" \
  "v_cvt_pk_bf16_f32 v216, v204, v205\n" \
  "v_cvt_pk_bf16_f32 v217, v206, v207\n" \
  "v_cvt_pk_bf16_f32 v218, v208, v209\n" \
  "v_cvt_pk_bf16_f32 v219, v210, v211\n" \
  "v_mul_f32 v204, v80, v80\n" \
  "v_mul_f32 v205, v81, v81\n" \
  "v_mul_f32 v206, v82, v82\n" \
  "v_mul_f32 v207, v83, v83\n" \
  "v_mul_f32 v208, v84, v84\n" \
  "v_mul_f32 v209, v85, v85\n" \
  "v_mul_f32 v210, v86, v86\n" \
  "v_mul_f32 v211, v87, v87\n" \
  "v_mfma_f32_16x16x32_bf16 v[224:227], v[36:39], v[216:219], v[224:227]\n" \
  "v_fmamk_f32 v204, v204, 0xbdd2d3e8, v200\n" \
  "v_fmamk_f32 v205, v205, 0xbdd2d3e8, v200\n" \
  "v_fmamk_f32 v206, v206, 0xbdd2d3e8, v200\n" \
  "v_fmamk_f32 v207, v207, 0xbdd2d3e8, v200\n" \
  "v_fmamk_f32 v208, v208, 0xbdd2d3e8, v200\n" \
  "v_fmamk_f32 v209, v209, 0xbdd2d3e8, v200\n" \
  "v_fmamk_f32 v210, v210, 0xbdd2d3e8, v200\n" \
  "v_fmamk_f32 v211, v211, 0xbdd2d3e8, v200\n" \
  "v_mfma_f32_16x16x32_bf16 v[96:99], v[44:47], v[164:167], v[96:99]\n" \
  "v_mfma_f32_16x16x32_bf16 v[100:103], v[42:45], v[164:167], v[100:103]\n" \
  "v_mul_f32 v204, v204, v80\n" \
  "v_mul_f32 v205, v205, v81\n" \
  "v_mul_f32 v206, v206, v82\n" \
  "v_mul_f32 v207, v207, v83\n" \
  "v_mul_f32 v208, v208, v84\n" \
  "v_mul_f32 v209, v209, v85\n" \
  "v_mul_f32 v210, v210, v86\n" \
  "v_mul_f32 v211, v211, v87\n" \
  "v_exp_f32 v204, v204\n" \
  "v_exp_f32 v205, v205\n" \
  "v_exp_f32 v206, v206\n" \
  "v_exp_f32 v207, v207\n" \
  "v_exp_f32 v208, v208\n" \
  "v_exp_f32 v209, v209\n" \
  "v_exp_f32 v210, v210\n" \
  "v_exp_f32 v211, v211\n" \
  "v_add_f32 v204, 1.0, v204\n" \
  "v_add_f32 v205, 1.0, v205\n" \
  "v_add_f32 v206, 1.0, v206\n" \
  "v_add_f32 v207, 1.0, v207\n" \
  "v_add_f32 v208, 1.0, v208\n" \
  "v_add_f32 v209, 1.0, v209\n" \
  "v_add_f32 v210, 1.0, v210\n" \
  "v_add_f32 v211, 1.0, v211\n" \
  "v_rcp_f32 v204, v204\n" \
  "v_rcp_f32 v205, v205\n" \
  "v_rcp_f32 v206, v206\n" \
  "v_rcp_f32 v207, v207\n" \
  "v_rcp_f32 v208, v208\n" \
  "v_rcp_f32 v209, v209\n" \
  "v_rcp_f32 v210, v210\n" \
  "v_rcp_f32 v211, v211\n" \
  "v_mul_f32 v204, v204, v80\n" \
  "v_mul_f32 v205, v205, v81\n" \
  "v_mul_f32 v206, v206, v82\n" \
  "v_mul_f32 v207, v207, v83\n" \
  "v_mul_f32 v208, v208, v84\n" \
  "v_mul_f32 v209, v209, v85\n" \
  "v_mul_f32 v210, v210, v86\n" \
  "v_mul_f32 v211, v211, v87\n" \
  "v_cvt_pk_bf16_f32 v212, v204, v205\n" \
  "v_cvt_pk_bf16_f32 v213, v206, v207\n" \
  "v_cvt_pk_bf16_f32 v214, v208, v209\n" \
  "v_cvt_pk_bf16_f32 v215, v210, v211\n" \
  "v_mul_f32 v204, v88, v88\n" \
  "v_mul_f32 v205, v89, v89\n" \
  "v_mul_f32 v206, v90, v90\n" \
  "v_mul_f32 v207, v91, v91\n" \
  "v_mul_f32 v208, v92, v92\n" \
  "v_mul_f32 v209, v93, v93\n" \
  "v_mul_f32 v210, v94, v94\n" \
  "v_mul_f32 v211, v95, v95\n" \
  "v_mfma_f32_16x16x32_bf16 v[228:231], v[32:35], v[212:215], 0\n" \
  "v_fmamk_f32 v204, v204, 0xbdd2d3e8, v200\n" \
  "v_fmamk_f32 v205, v205, 0xbdd2d3e8, v200\n" \
  "v_fmamk_f32 v206, v206, 0xbdd2d3e8, v200\n" \
  "v_fmamk_f32 v207, v207, 0xbdd2d3e8, v200\n" \
  "v_fmamk_f32 v208, v208, 0xbdd2d3e8, v200\n" \
  "v_fmamk_f32 v209, v209, 0xbdd2d3e8, v200\n" \
  "v_fmamk_f32 v210, v210, 0xbdd2d3e8, v200\n" \
  "v_fmamk_f32 v211, v211, 0xbdd2d3e8, v200\n" \
  "v_mfma_f32_16x16x32_bf16 v[104:107], v[44:47], v[172:175], v[104:107]\n" \
  "v_mfma_f32_16x16x32_bf16 v[108:111], v[42:45], v[172:175], v[108:111]\n" \
  "v_mul_f32 v204, v204, v88\n" \
  "v_mul_f32 v205, v205, v89\n" \
  "v_mul_f32 v206, v206, v90\n" \
  "v_mul_f32 v207, v207, v91\n" \
  "v_mul_f32 v208, v208, v92\n" \
  "v_mul_f32 v209, v209, v93\n" \
  "v_mul_f32 v210, v210, v94\n" \
  "v_mul_f32 v211, v211, v95\n" \
  "v_exp_f32 v204, v204\n" \
  "v_exp_f32 v205, v205\n" \
  "v_exp_f32 v206, v206\n" \
  "v_exp_f32 v207, v207\n" \
  "v_exp_f32 v208, v208\n" \
  "v_exp_f32 v209, v209\n" \
  "v_exp_f32 v210, v210\n" \
  "v_exp_f32 v211, v211\n" \
  "v_add_f32 v204, 1.0, v204\n" \
  "v_add_f32 v205, 1.0, v205\n" \
  "v_add_f32 v206, 1.0, v206\n" \
  "v_add_f32 v207, 1.0, v207\n" \
  "v_add_f32 v208, 1.0, v208\n" \
  "v_add_f32 v209, 1.0, v209\n" \
  "v_add_f32 v210, 1.0, v210\n" \
  "v_add_f32 v211, 1.0, v211\n" \
  "v_rcp_f32 v204, v204\n" \
  "v_rcp_f32 v205, v205\n" \
  "v_rcp_f32 v206, v206\n" \
  "v_rcp_f32 v207, v207\n" \
  "v_rcp_f32 v208, v208\n" \
  "v_rcp_f32 v209, v209\n" \
  "v_rcp_f32 v210, v210\n" \
  "v_rcp_f32 v211, v211\n" \
  "v_mul_f32 v204, v204, v88\n" \
  "v_mul_f32 v205, v205, v89\n" \
  "v_mul_f32 v206, v206, v90\n" \
  "v_mul_f32 v207, v207, v91\n" \
  "v_mul_f32 v208, v208, v92\n" \
  "v_mul_f32 v209, v209, v93\n" \
  "v_mul_f32 v210, v210, v94\n" \
  "v_mul_f32 v211, v211, v95\n" \
  "v_cvt_pk_bf16_f32 v216, v204, v205\n" \
  "v_cvt_pk_bf16_f32 v217, v206, v207\n" \
  "v_cvt_pk_bf16_f32 v218, v208, v209\n" \
  "v_cvt_pk_bf16_f32 v219, v210, v211\n" \
  "v_mul_f32 v204, v96, v96\n" \
  "v_mul_f32 v205, v97, v97\n" \
  "v_mul_f32 v206, v98, v98\n" \
  "v_mul_f32 v207, v99, v99\n" \
  "v_mul_f32 v208, v100, v100\n" \
  "v_mul_f32 v209, v101, v101\n" \
  "v_mul_f32 v210, v102, v102\n" \
  "v_mul_f32 v211, v103, v103\n" \
  "v_mfma_f32_16x16x32_bf16 v[228:231], v[36:39], v[216:219], v[228:231]\n" \
  "v_fmamk_f32 v204, v204, 0xbdd2d3e8, v200\n" \
  "v_fmamk_f32 v205, v205, 0xbdd2d3e8, v200\n" \
  "v_fmamk_f32 v206, v206, 0xbdd2d3e8, v200\n" \
  "v_fmamk_f32 v207, v207, 0xbdd2d3e8, v200\n" \
  "v_fmamk_f32 v208, v208, 0xbdd2d3e8, v200\n" \
  "v_fmamk_f32 v209, v209, 0xbdd2d3e8, v200\n" \
  "v_fmamk_f32 v210, v210, 0xbdd2d3e8, v200\n" \
  "v_fmamk_f32 v211, v211, 0xbdd2d3e8, v200\n" \
  "v_mfma_f32_16x16x32_bf16 v[112:115], v[44:47], v[180:183], v[112:115]\n" \
  "v_mfma_f32_16x16x32_bf16 v[116:119], v[42:45], v[180:183], v[116:119]\n" \
  "v_mul_f32 v204, v204, v96\n" \
  "v_mul_f32 v205, v205, v97\n" \
  "v_mul_f32 v206, v206, v98\n" \
  "v_mul_f32 v207, v207, v99\n" \
  "v_mul_f32 v208, v208, v100\n" \
  "v_mul_f32 v209, v209, v101\n" \
  "v_mul_f32 v210, v210, v102\n" \
  "v_mul_f32 v211, v211, v103\n" \
  "v_exp_f32 v204, v204\n" \
  "v_exp_f32 v205, v205\n" \
  "v_exp_f32 v206, v206\n" \
  "v_exp_f32 v207, v207\n" \
  "v_exp_f32 v208, v208\n" \
  "v_exp_f32 v209, v209\n" \
  "v_exp_f32 v210, v210\n" \
  "v_exp_f32 v211, v211\n" \
  "v_add_f32 v204, 1.0, v204\n" \
  "v_add_f32 v205, 1.0, v205\n" \
  "v_add_f32 v206, 1.0, v206\n" \
  "v_add_f32 v207, 1.0, v207\n" \
  "v_add_f32 v208, 1.0, v208\n" \
  "v_add_f32 v209, 1.0, v209\n" \
  "v_add_f32 v210, 1.0, v210\n" \
  "v_add_f32 v211, 1.0, v211\n" \
  "v_rcp_f32 v204, v204\n" \
  "v_rcp_f32 v205, v205\n" \
  "v_rcp_f32 v206, v206\n" \
  "v_rcp_f32 v207, v207\n" \
  "v_rcp_f32 v208, v208\n" \
  "v_rcp_f32 v209, v209\n" \
  "v_rcp_f32 v210, v210\n" \
  "v_rcp_f32 v211, v211\n" \
  "v_mul_f32 v204, v204, v96\n" \
  "v_mul_f32 v205, v205, v97\n" \
  "v_mul_f32 v206, v206, v98\n" \
  "v_mul_f32 v207, v207, v99\n" \
  "v_mul_f32 v208, v208, v100\n" \
  "v_mul_f32 v209, v209, v101\n" \
  "v_mul_f32 v210, v210, v102\n" \
  "v_mul_f32 v211, v211, v103\n" \
  "v_cvt_pk_bf16_f32 v212, v204, v205\n" \
  "v_cvt_pk_bf16_f32 v213, v206, v207\n" \
  "v_cvt_pk_bf16_f32 v214, v208, v209\n" \
  "v_cvt_pk_bf16_f32 v215, v210, v211\n" \
  "v_mul_f32 v204, v104, v104\n" \
  "v_mul_f32 v205, v105, v105\n" \
  "v_mul_f32 v206, v106, v106\n" \
  "v_mul_f32 v207, v107, v107\n" \
  "v_mul_f32 v208, v108, v108\n" \
  "v_mul_f32 v209, v109, v109\n" \
  "v_mul_f32 v210, v110, v110\n" \
  "v_mul_f32 v211, v111, v111\n" \
  "v_mfma_f32_16x16x32_bf16 v[232:235], v[32:35], v[212:215], 0\n" \
  "v_fmamk_f32 v204, v204, 0xbdd2d3e8, v200\n" \
  "v_fmamk_f32 v205, v205, 0xbdd2d3e8, v200\n" \
  "v_fmamk_f32 v206, v206, 0xbdd2d3e8, v200\n" \
  "v_fmamk_f32 v207, v207, 0xbdd2d3e8, v200\n" \
  "v_fmamk_f32 v208, v208, 0xbdd2d3e8, v200\n" \
  "v_fmamk_f32 v209, v209, 0xbdd2d3e8, v200\n" \
  "v_fmamk_f32 v210, v210, 0xbdd2d3e8, v200\n" \
  "v_fmamk_f32 v211, v211, 0xbdd2d3e8, v200\n" \
  "v_mfma_f32_16x16x32_bf16 v[120:123], v[44:47], v[188:191], v[120:123]\n" \
  "v_mfma_f32_16x16x32_bf16 v[124:127], v[42:45], v[188:191], v[124:127]\n" \
  "v_mul_f32 v204, v204, v104\n" \
  "v_mul_f32 v205, v205, v105\n" \
  "v_mul_f32 v206, v206, v106\n" \
  "v_mul_f32 v207, v207, v107\n" \
  "v_mul_f32 v208, v208, v108\n" \
  "v_mul_f32 v209, v209, v109\n" \
  "v_mul_f32 v210, v210, v110\n" \
  "v_mul_f32 v211, v211, v111\n" \
  "v_exp_f32 v204, v204\n" \
  "v_exp_f32 v205, v205\n" \
  "v_exp_f32 v206, v206\n" \
  "v_exp_f32 v207, v207\n" \
  "v_exp_f32 v208, v208\n" \
  "v_exp_f32 v209, v209\n" \
  "v_exp_f32 v210, v210\n" \
  "v_exp_f32 v211, v211\n" \
  "v_add_f32 v204, 1.0, v204\n" \
  "v_add_f32 v205, 1.0, v205\n" \
  "v_add_f32 v206, 1.0, v206\n" \
  "v_add_f32 v207, 1.0, v207\n" \
  "v_add_f32 v208, 1.0, v208\n" \
  "v_add_f32 v209, 1.0, v209\n" \
  "v_add_f32 v210, 1.0, v210\n" \
  "v_add_f32 v211, 1.0, v211\n" \
  "v_rcp_f32 v204, v204\n" \
  "v_rcp_f32 v205, v205\n" \
  "v_rcp_f32 v206, v206\n" \
  "v_rcp_f32 v207, v207\n" \
  "v_rcp_f32 v208, v208\n" \
  "v_rcp_f32 v209, v209\n" \
  "v_rcp_f32 v210, v210\n" \
  "v_rcp_f32 v211, v211\n" \
  "v_mul_f32 v204, v204, v104\n" \
  "v_mul_f32 v205, v205, v105\n" \
  "v_mul_f32 v206, v206, v106\n" \
  "v_mul_f32 v207, v207, v107\n" \
  "v_mul_f32 v208, v208, v108\n" \
  "v_mul_f32 v209, v209, v109\n" \
  "v_mul_f32 v210, v210, v110\n" \
  "v_mul_f32 v211, v211, v111\n" \
  "v_cvt_pk_bf16_f32 v216, v204, v205\n" \
  "v_cvt_pk_bf16_f32 v217, v206, v207\n" \
  "v_cvt_pk_bf16_f32 v218, v208, v209\n" \
  "v_cvt_pk_bf16_f32 v219, v210, v211\n" \
  "v_mul_f32 v204, v112, v112\n" \
  "v_mul_f32 v205, v113, v113\n" \
  "v_mul_f32 v206, v114, v114\n" \
  "v_mul_f32 v207, v115, v115\n" \
  "v_mul_f32 v208, v116, v116\n" \
  "v_mul_f32 v209, v117, v117\n" \
  "v_mul_f32 v210, v118, v118\n" \
  "v_mul_f32 v211, v119, v119\n" \
  "v_mfma_f32_16x16x32_bf16 v[232:235], v[36:39], v[216:219], v[232:235]\n" \
  "v_fmamk_f32 v204, v204, 0xbdd2d3e8, v200\n" \
  "v_fmamk_f32 v205, v205, 0xbdd2d3e8, v200\n" \
  "v_fmamk_f32 v206, v206, 0xbdd2d3e8, v200\n" \
  "v_fmamk_f32 v207, v207, 0xbdd2d3e8, v200\n" \
  "v_fmamk_f32 v208, v208, 0xbdd2d3e8, v200\n" \
  "v_fmamk_f32 v209, v209, 0xbdd2d3e8, v200\n" \
  "v_fmamk_f32 v210, v210, 0xbdd2d3e8, v200\n" \
  "v_fmamk_f32 v211, v211, 0xbdd2d3e8, v200\n" \
  "v_mul_f32 v204, v204, v112\n" \
  "v_mul_f32 v205, v205, v113\n" \
  "v_mul_f32 v206, v206, v114\n" \
  "v_mul_f32 v207, v207, v115\n" \
  "v_mul_f32 v208, v208, v116\n" \
  "v_mul_f32 v209, v209, v117\n" \
  "v_mul_f32 v210, v210, v118\n" \
  "v_mul_f32 v211, v211, v119\n" \
  "v_exp_f32 v204, v204\n" \
  "v_exp_f32 v205, v205\n" \
  "v_exp_f32 v206, v206\n" \
  "v_exp_f32 v207, v207\n" \
  "v_exp_f32 v208, v208\n" \
  "v_exp_f32 v209, v209\n" \
  "v_exp_f32 v210, v210\n" \
  "v_exp_f32 v211, v211\n" \
  "v_add_f32 v204, 1.0, v204\n" \
  "v_add_f32 v205, 1.0, v205\n" \
  "v_add_f32 v206, 1.0, v206\n" \
  "v_add_f32 v207, 1.0, v207\n" \
  "v_add_f32 v208, 1.0, v208\n" \
  "v_add_f32 v209, 1.0, v209\n" \
  "v_add_f32 v210, 1.0, v210\n" \
  "v_add_f32 v211, 1.0, v211\n" \
  "v_rcp_f32 v204, v204\n" \
  "v_rcp_f32 v205, v205\n" \
  "v_rcp_f32 v206, v206\n" \
  "v_rcp_f32 v207, v207\n" \
  "v_rcp_f32 v208, v208\n" \
  "v_rcp_f32 v209, v209\n" \
  "v_rcp_f32 v210, v210\n" \
  "v_rcp_f32 v211, v211\n" \
  "v_mul_f32 v204, v204, v112\n" \
  "v_mul_f32 v205, v205, v113\n" \
  "v_mul_f32 v206, v206, v114\n" \
  "v_mul_f32 v207, v207, v115\n" \
  "v_mul_f32 v208, v208, v116\n" \
  "v_mul_f32 v209, v209, v117\n" \
  "v_mul_f32 v210, v210, v118\n" \
  "v_mul_f32 v211, v211, v119\n" \
  "v_cvt_pk_bf16_f32 v212, v204, v205\n" \
  "v_cvt_pk_bf16_f32 v213, v206, v207\n" \
  "v_cvt_pk_bf16_f32 v214, v208, v209\n" \
  "v_cvt_pk_bf16_f32 v215, v210, v211\n" \
  "v_mul_f32 v204, v120, v120\n" \
  "v_mul_f32 v205, v121, v121\n" \
  "v_mul_f32 v206, v122, v122\n" \
  "v_mul_f32 v207, v123, v123\n" \
  "v_mul_f32 v208, v124, v124\n" \
  "v_mul_f32 v209, v125, v125\n" \
  "v_mul_f32 v210, v126, v126\n" \
  "v_mul_f32 v211, v127, v127\n" \
  "v_mfma_f32_16x16x32_bf16 v[236:239], v[32:35], v[212:215], 0\n" \
  "v_fmamk_f32 v204, v204, 0xbdd2d3e8, v200\n" \
  "v_fmamk_f32 v205, v205, 0xbdd2d3e8, v200\n" \
  "v_fmamk_f32 v206, v206, 0xbdd2d3e8, v200\n" \
  "v_fmamk_f32 v207, v207, 0xbdd2d3e8, v200\n" \
  "v_fmamk_f32 v208, v208, 0xbdd2d3e8, v200\n" \
  "v_fmamk_f32 v209, v209, 0xbdd2d3e8, v200\n" \
  "v_fmamk_f32 v210, v210, 0xbdd2d3e8, v200\n" \
  "v_fmamk_f32 v211, v211, 0xbdd2d3e8, v200\n" \
  "v_mul_f32 v204, v204, v120\n" \
  "v_mul_f32 v205, v205, v121\n" \
  "v_mul_f32 v206, v206, v122\n" \
  "v_mul_f32 v207, v207, v123\n" \
  "v_mul_f32 v208, v208, v124\n" \
  "v_mul_f32 v209, v209, v125\n" \
  "v_mul_f32 v210, v210, v126\n" \
  "v_mul_f32 v211, v211, v127\n" \
  "v_exp_f32 v204, v204\n" \
  "v_exp_f32 v205, v205\n" \
  "v_exp_f32 v206, v206\n" \
  "v_exp_f32 v207, v207\n" \
  "v_exp_f32 v208, v208\n" \
  "v_exp_f32 v209, v209\n" \
  "v_exp_f32 v210, v210\n" \
  "v_exp_f32 v211, v211\n" \
  "v_add_f32 v204, 1.0, v204\n" \
  "v_add_f32 v205, 1.0, v205\n" \
  "v_add_f32 v206, 1.0, v206\n" \
  "v_add_f32 v207, 1.0, v207\n" \
  "v_add_f32 v208, 1.0, v208\n" \
  "v_add_f32 v209, 1.0, v209\n" \
  "v_add_f32 v210, 1.0, v210\n" \
  "v_add_f32 v211, 1.0, v211\n" \
  "v_rcp_f32 v204, v204\n" \
  "v_rcp_f32 v205, v205\n" \
  "v_rcp_f32 v206, v206\n" \
  "v_rcp_f32 v207, v207\n" \
  "v_rcp_f32 v208, v208\n" \
  "v_rcp_f32 v209, v209\n" \
  "v_rcp_f32 v210, v210\n" \
  "v_rcp_f32 v211, v211\n" \
  "v_mul_f32 v204, v204, v120\n" \
  "v_mul_f32 v205, v205, v121\n" \
  "v_mul_f32 v206, v206, v122\n" \
  "v_mul_f32 v207, v207, v123\n" \
  "v_mul_f32 v208, v208, v124\n" \
  "v_mul_f32 v209, v209, v125\n" \
  "v_mul_f32 v210, v210, v126\n" \
  "v_mul_f32 v211, v211, v127\n" \
  "v_cvt_pk_bf16_f32 v216, v204, v205\n" \
  "v_cvt_pk_bf16_f32 v217, v206, v207\n" \
  "v_cvt_pk_bf16_f32 v218, v208, v209\n" \
  "v_cvt_pk_bf16_f32 v219, v210, v211\n" \
  "s_nop 1\n" \
  "v_mfma_f32_16x16x32_bf16 v[236:239], v[36:39], v[216:219], v[236:239]\n" \
  "v_mov_b32 v192, v224\n" \
  "s_mov_b32 exec_lo, 0xffff0000\n" \
  "s_mov_b32 exec_hi, 0\n" \
  "v_mov_b32 v192, v228\n" \
  "s_mov_b32 exec_lo, 0\n" \
  "s_mov_b32 exec_hi, 0xffff\n" \
  "v_mov_b32 v192, v232\n" \
  "s_mov_b32 exec_hi, 0xffff0000\n" \
  "s_nop 3\n" \
  "v_mov_b32 v192, v236\n" \
  "s_mov_b64 exec, -1\n"
#define GEMM_CONS_CLOB "v193", "v194", "v195", "v196", "v197", "v198", "v199", "v200", "v201", "v202", "v203", "v204", "v205", "v206", "v207", "v208", "v209", "v210", "v211", "v212", "v213", "v214", "v215", "v216", "v217", "v218", "v219", "v220", "v221", "v222", "v223", "v224", "v225", "v226", "v227", "v228", "v229", "v230", "v231", "v232", "v233", "v234", "v235", "v236", "v237", "v238", "v239", "v240", "v241", "v242", "v243", "v244", "v245", "v246", "v247", "v248", "v249", "v250", "v251", "v252", "v253", "v254", "v255"
#define GEMM_PROD_ASM \
  "ds_read_b32 v96, %[r0] offset:0\n" \
  "ds_read_b32 v97, %[r0] offset:512\n" \
  "ds_read_b32 v98, %[r0] offset:1024\n" \
  "ds_read_b32 v99, %[r0] offset:1536\n" \
  "ds_read_b32 v100, %[r0] offset:2048\n" \
  "ds_read_b32 v101, %[r0] offset:2560\n" \
  "ds_read_b32 v102, %[r0] offset:3072\n" \
  "ds_read_b32 v103, %[r0] offset:3584\n" \
  "ds_read_b32 v120, %[p0]\n" \
  "ds_read_b32 v121, %[p0] offset:1664\n" \
  "ds_read_b32 v122, %[p2]\n" \
  "v_mov_b32 v115, %[l16]\n" \
  "v_mov_b32 v116, -1\n" \
  "v_mov_b32 v117, -1\n" \
  "v_mov_b32 v118, -1\n" \
  "v_mov_b32 v119, -1\n" \
  "s_waitcnt lgkmcnt(3)\n" \
  "v_lshl_add_u32 v96, v96, 8, %[a0]\n" \
  "v_lshl_add_u32 v97, v97, 8, %[a1]\n" \
  "v_lshl_add_u32 v98, v98, 8, %[a2]\n" \
  "v_lshl_add_u32 v99, v99, 8, %[a3]\n" \
  "v_lshl_add_u32 v100, v100, 8, %[a0]\n" \
  "v_lshl_add_u32 v101, v101, 8, %[a1]\n" \
  "v_lshl_add_u32 v102, v102, 8, %[a2]\n" \
  "v_lshl_add_u32 v103, v103, 8, %[a3]\n" \
  "s_add_u32 m0, %[pa], 0\n" \
  "s_nop 0\n" \
  "buffer_load_dwordx4 v96, %[rsa], 0 offen lds\n" \
  "buffer_load_dwordx4 v97, %[rsa], 0 offen offset:1024 lds\n" \
  "buffer_load_dwordx4 v98, %[rsa], 0 offen offset:2048 lds\n" \
  "buffer_load_dwordx4 v99, %[rsa], 0 offen offset:3072 lds\n" \
  "s_add_u32 m0, m0, 0x1000\n" \
  "s_nop 0\n" \
  "buffer_load_dwordx4 v100, %[rsa], 0 offen lds\n" \
  "buffer_load_dwordx4 v101, %[rsa], 0 offen offset:1024 lds\n" \
  "buffer_load_dwordx4 v102, %[rsa], 0 offen offset:2048 lds\n" \
  "buffer_load_dwordx4 v103, %[rsa], 0 offen offset:3072 lds\n" \
  "ds_read_b128 v[144:147], %[ra] offset:0\n" \
  "ds_read_b128 v[148:151], %[ra] offset:16\n" \
  "ds_read_b128 v[152:155], %[ra] offset:32\n" \
  "ds_read_b64 v[156:157], %[ra] offset:48\n" \
  "ds_read_b128 v[158:161], %[ra] offset:512\n" \
  "ds_read_b128 v[162:165], %[ra] offset:528\n" \
  "ds_read_b128 v[166:169], %[ra] offset:544\n" \
  "ds_read_b64 v[170:171], %[ra] offset:560\n" \
  "ds_read_b128 v[172:175], %[ra] offset:1024\n" \
  "ds_read_b128 v[176:179], %[ra] offset:1040\n" \
  "ds_read_b128 v[180:183], %[ra] offset:1056\n" \
  "ds_read_b64 v[184:185], %[ra] offset:1072\n" \
  "ds_read_b128 v[186:189], %[ra] offset:1536\n" \
  "ds_read_b128 v[190:193], %[ra] offset:1552\n" \
  "ds_read_b128 v[194:197], %[ra] offset:1568\n" \
  "ds_read_b64 v[198:199], %[ra] offset:1584\n" \
  "ds_read_b128 v[200:203], %[ra] offset:2048\n" \
  "ds_read_b128 v[204:207], %[ra] offset:2064\n" \
  "ds_read_b128 v[208:211], %[ra] offset:2080\n" \
  "ds_read_b64 v[212:213], %[ra] offset:2096\n" \
  "ds_read_b128 v[214:217], %[ra] offset:2560\n" \
  "ds_read_b128 v[218:221], %[ra] offset:2576\n" \
  "ds_read_b128 v[222:225], %[ra] offset:2592\n" \
  "ds_read_b64 v[226:227], %[ra] offset:2608\n" \
  "ds_read_b128 v[228:231], %[ra] offset:3072\n" \
  "ds_read_b128 v[232:235], %[ra] offset:3088\n" \
  "ds_read_b128 v[236:239], %[ra] offset:3104\n" \
  "ds_read_b64 v[240:241], %[ra] offset:3120\n" \
  "ds_read_b128 v[242:245], %[ra] offset:3584\n" \
  "ds_read_b128 v[246:249], %[ra] offset:3600\n" \
  "ds_read_b128 v[250:253], %[ra] offset:3616\n" \
  "ds_read_b64 v[254:255], %[ra] offset:3632\n" \
  "v_mov_b32 v123, 0\n" \
  "s_waitcnt lgkmcnt(0)\n" \
  "ds_write_b32 %[zc], v123\n" \
  "v_readlane_b32 s84, v120, %[pf]\n" \
  "v_readlane_b32 s85, v121, %[pf]\n" \
  "v_readlane_b32 s86, v122, %[pf]\n" \
  "s_min_u32 s84, s84, 0x1869f\n" \
  "s_lshl_b32 s84, s84, 8\n" \
  "s_min_u32 s85, s85, 0x1869f\n" \
  "s_lshl_b32 s85, s85, 8\n" \
  "s_min_u32 s86, s86, 0x1869f\n" \
  "s_lshl_b32 s86, s86, 8\n" \
  "s_load_dword s87, %[tab], s84 offset:128\n" \
  "s_load_dword s87, %[tab], s84 offset:192\n" \
  "s_load_dword s87, %[tab], s85 offset:128\n" \
  "s_load_dword s87, %[tab], s85 offset:192\n" \
  "s_load_dword s87, %[tab], s86 offset:128\n" \
  "s_load_dword s87, %[tab], s86 offset:192\n" \
  "s_add_u32 %[pf], %[pf], 1\n" \
  "s_cmp_eq_u32 %[pf], 14\n" \
  "s_cselect_b32 %[pf], 0, %[pf]\n" \
  "v_readlane_b32 s84, v120, %[pf]\n" \
  "v_readlane_b32 s85, v121, %[pf]\n" \
  "v_readlane_b32 s86, v122, %[pf]\n" \
  "s_min_u32 s84, s84, 0x1869f\n" \
  "s_lshl_b32 s84, s84, 8\n" \
  "s_min_u32 s85, s85, 0x1869f\n" \
  "s_lshl_b32 s85, s85, 8\n" \
  "s_min_u32 s86, s86, 0x1869f\n" \
  "s_lshl_b32 s86, s86, 8\n" \
  "s_load_dword s87, %[tab], s84 offset:0\n" \
  "s_load_dword s87, %[tab], s84 offset:64\n" \
  "s_load_dword s87, %[tab], s85 offset:0\n" \
  "s_load_dword s87, %[tab], s85 offset:64\n" \
  "s_load_dword s87, %[tab], s86 offset:0\n" \
  "s_load_dword s87, %[tab], s86 offset:64\n" \
  "s_add_u32 %[sob], %[sob], 0x68000\n" \
  "s_add_u32 m0, %[pb], 0xc000\n" \
  "s_nop 0\n" \
  "buffer_load_dwordx4 %[b0], %[rsb], %[sob] offen lds\n" \
  "buffer_load_dwordx4 %[b1], %[rsb], %[sob] offen offset:1024 lds\n" \
  "buffer_load_dwordx4 %[b2], %[rsb], %[sob] offen offset:2048 lds\n" \
  "buffer_load_dwordx4 %[b3], %[rsb], %[sob] offen offset:3072 lds\n" \
  "buffer_load_dword v143, %[tv], %[rsb], %[sob] offen\n" \
  "s_add_u32 m0, %[pa], 0xc000\n" \
  "s_nop 0\n" \
  "buffer_load_dwordx4 v96, %[rsa], %[h] offen lds\n" \
  "buffer_load_dwordx4 v97, %[rsa], %[h] offen offset:1024 lds\n" \
  "buffer_load_dwordx4 v98, %[rsa], %[h] offen offset:2048 lds\n" \
  "buffer_load_dwordx4 v99, %[rsa], %[h] offen offset:3072 lds\n" \
  "s_add_u32 m0, m0, 0x1000\n" \
  "s_nop 0\n" \
  "buffer_load_dwordx4 v100, %[rsa], %[h] offen lds\n" \
  "buffer_load_dwordx4 v101, %[rsa], %[h] offen offset:1024 lds\n" \
  "buffer_load_dwordx4 v102, %[rsa], %[h] offen offset:2048 lds\n" \
  "buffer_load_dwordx4 v103, %[rsa], %[h] offen offset:3072 lds\n" \
  "buffer_load_dword v128, %[xa], %[rsi], %[xs] offen offset:0\n" \
  "buffer_load_dword v129, %[xa], %[rsi], %[xs] offen offset:32\n" \
  "buffer_load_dword v130, %[xa], %[rsi], %[xs] offen offset:64\n" \
  "buffer_load_dword v131, %[xa], %[rsi], %[xs] offen offset:96\n" \
  "global_load_dwordx2 v[132:133], %[wa], %[wco] offset:0\n" \
  "global_load_dwordx2 v[134:135], %[wa], %[wco] offset:16\n" \
  "global_load_dwordx2 v[136:137], %[wa], %[wco] offset:32\n" \
  "global_load_dwordx2 v[138:139], %[wa], %[wco] offset:48\n" \
  "s_add_u32 %[sob], %[sob], 0x68000\n" \
  "s_mov_b32 %[slot], 0x18000\n" \
  "s_mov_b32 s82, %[pf]\n" \
  "s_waitcnt vmcnt(21)\n" \
  "s_load_dword s87, %[tab], s84 offset:128\n" \
  "s_load_dword s87, %[tab], s84 offset:192\n" \
  "s_load_dword s87, %[tab], s85 offset:128\n" \
  "s_load_dword s87, %[tab], s85 offset:192\n" \
  "s_load_dword s87, %[tab], s86 offset:128\n" \
  "s_load_dword s87, %[tab], s86 offset:192\n" \
  "4:\n" \
  "s_barrier\n" \
  "s_cmp_lg_u32 %[rem], 2\n" \
  "s_cbranch_scc1 42f\n" \
  "s_waitcnt vmcnt(0)\n" \
  "global_store_dwordx2 %[zv], v[126:127], %[rme] sc1\n" \
  "42:\n" \
  "s_add_u32 m0, %[pb], %[slot]\n" \
  "s_nop 0\n" \
  "buffer_load_dwordx4 %[b0], %[rsb], %[sob] offen lds\n" \
  "buffer_load_dwordx4 %[b1], %[rsb], %[sob] offen offset:1024 lds\n" \
  "buffer_load_dwordx4 %[b2], %[rsb], %[sob] offen offset:2048 lds\n" \
  "buffer_load_dwordx4 %[b3], %[rsb], %[sob] offen offset:3072 lds\n" \
  "s_cmp_lt_u32 %[rem], 4\n" \
  "s_cbranch_scc1 41f\n" \
  "buffer_store_dwordx4 v[116:119], v115, %[rsx], %[sme] offen sc1\n" \
  "v_add_u32 v115, 0x400, v115\n" \
  "41:\n" \
  "s_set_gpr_idx_on %[pf], gpr_idx(SRC0)\n" \
  "s_nop 0\n" \
  "v_lshl_add_u32 v96, v144, 8, %[a0]\n" \
  "v_lshl_add_u32 v97, v158, 8, %[a1]\n" \
  "v_lshl_add_u32 v98, v172, 8, %[a2]\n" \
  "v_lshl_add_u32 v99, v186, 8, %[a3]\n" \
  "v_lshl_add_u32 v100, v200, 8, %[a0]\n" \
  "v_lshl_add_u32 v101, v214, 8, %[a1]\n" \
  "v_lshl_add_u32 v102, v228, 8, %[a2]\n" \
  "v_lshl_add_u32 v103, v242, 8, %[a3]\n" \
  "s_set_gpr_idx_off\n" \
  "s_mov_b32 s83, %[slot]\n" \
  "s_sub_u32 %[rem], %[rem], 1\n" \
  "s_add_u32 %[pf], %[pf], 1\n" \
  "s_cmp_eq_u32 %[pf], 14\n" \
  "s_cselect_b32 %[pf], 0, %[pf]\n" \
  "s_add_u32 %[slot], %[slot], 0xc000\n" \
  "s_add_u32 %[sob], %[sob], 0x68000\n" \
  "s_cmp_eq_u32 %[slot], 0x24000\n" \
  "s_cselect_b32 %[slot], 0, %[slot]\n" \
  "s_add_u32 s82, s82, 1\n" \
  "s_cmp_eq_u32 s82, 14\n" \
  "s_cselect_b32 s82, 0, s82\n" \
  "v_readlane_b32 s84, v120, s82\n" \
  "v_readlane_b32 s85, v121, s82\n" \
  "v_readlane_b32 s86, v122, s82\n" \
  "s_min_u32 s84, s84, 0x1869f\n" \
  "s_lshl_b32 s84, s84, 8\n" \
  "s_min_u32 s85, s85, 0x1869f\n" \
  "s_lshl_b32 s85, s85, 8\n" \
  "s_min_u32 s86, s86, 0x1869f\n" \
  "s_lshl_b32 s86, s86, 8\n" \
  "s_load_dword s87, %[tab], s84 offset:0\n" \
  "s_load_dword s87, %[tab], s84 offset:64\n" \
  "s_load_dword s87, %[tab], s85 offset:0\n" \
  "s_load_dword s87, %[tab], s85 offset:64\n" \
  "s_load_dword s87, %[tab], s86 offset:0\n" \
  "s_load_dword s87, %[tab], s86 offset:64\n" \
  "s_add_u32 m0, %[pa], s83\n" \
  "s_nop 0\n" \
  "buffer_load_dwordx4 v96, %[rsa], 0 offen lds\n" \
  "buffer_load_dwordx4 v97, %[rsa], 0 offen offset:1024 lds\n" \
  "buffer_load_dwordx4 v98, %[rsa], 0 offen offset:2048 lds\n" \
  "buffer_load_dwordx4 v99, %[rsa], 0 offen offset:3072 lds\n" \
  "s_add_u32 m0, m0, 0x1000\n" \
  "s_nop 0\n" \
  "buffer_load_dwordx4 v100, %[rsa], 0 offen lds\n" \
  "buffer_load_dwordx4 v101, %[rsa], 0 offen offset:1024 lds\n" \
  "buffer_load_dwordx4 v102, %[rsa], 0 offen offset:2048 lds\n" \
  "buffer_load_dwordx4 v103, %[rsa], 0 offen offset:3072 lds\n" \
  "s_waitcnt vmcnt(12)\n" \
  "s_barrier\n" \
  "s_add_u32 m0, %[pb], %[slot]\n" \
  "s_nop 0\n" \
  "buffer_load_dwordx4 %[b0], %[rsb], %[sob] offen lds\n" \
  "buffer_load_dwordx4 %[b1], %[rsb], %[sob] offen offset:1024 lds\n" \
  "buffer_load_dwordx4 %[b2], %[rsb], %[sob] offen offset:2048 lds\n" \
  "buffer_load_dwordx4 %[b3], %[rsb], %[sob] offen offset:3072 lds\n" \
  "s_cmp_eq_u32 %[rem], 0\n" \
  "s_cbranch_scc1 30f\n" \
  "5:\n" \
  "s_mov_b32 s83, %[slot]\n" \
  "s_add_u32 %[slot], %[slot], 0xc000\n" \
  "s_add_u32 %[sob], %[sob], 0x68000\n" \
  "s_cmp_eq_u32 %[slot], 0x24000\n" \
  "s_cselect_b32 %[slot], 0, %[slot]\n" \
  "s_load_dword s87, %[tab], s84 offset:128\n" \
  "s_load_dword s87, %[tab], s84 offset:192\n" \
  "s_load_dword s87, %[tab], s85 offset:128\n" \
  "s_load_dword s87, %[tab], s85 offset:192\n" \
  "s_load_dword s87, %[tab], s86 offset:128\n" \
  "s_load_dword s87, %[tab], s86 offset:192\n" \
  "s_add_u32 m0, %[pa], s83\n" \
  "s_nop 0\n" \
  "buffer_load_dwordx4 v96, %[rsa], %[h] offen lds\n" \
  "buffer_load_dwordx4 v97, %[rsa], %[h] offen offset:1024 lds\n" \
  "buffer_load_dwordx4 v98, %[rsa], %[h] offen offset:2048 lds\n" \
  "buffer_load_dwordx4 v99, %[rsa], %[h] offen offset:3072 lds\n" \
  "s_add_u32 m0, m0, 0x1000\n" \
  "s_nop 0\n" \
  "buffer_load_dwordx4 v100, %[rsa], %[h] offen lds\n" \
  "buffer_load_dwordx4 v101, %[rsa], %[h] offen offset:1024 lds\n" \
  "buffer_load_dwordx4 v102, %[rsa], %[h] offen offset:2048 lds\n" \
  "buffer_load_dwordx4 v103, %[rsa], %[h] offen offset:3072 lds\n" \
  "s_cmp_lg_u32 %[rem], 0\n" \
  "s_waitcnt vmcnt(12)\n" \
  "s_cbranch_scc1 4b\n" \
  "s_branch 40f\n" \
  "30:\n" \
  "global_load_dwordx2 v[124:125], v140, %[rme] sc1\n" \
  "s_branch 5b\n" \
  "40:\n" \
  "s_movk_i32 %[rem], 0x7fff\n" \
  "36:\n" \
  "v_xor_b32 v123, v125, v127\n" \
  "v_xor_b32 v143, v124, v126\n" \
  "v_lshrrev_b32 v143, 24, v143\n" \
  "v_or_b32 v123, v123, v143\n" \
  "v_cmp_ne_u32 vcc, 0, v123\n" \
  "s_sub_u32 %[rem], %[rem], 1\n" \
  "s_cbranch_vccz 37f\n" \
  "s_cbranch_scc1 37f\n" \
  "s_sleep 8\n" \
  "global_load_dwordx2 v[124:125], v140, %[rme] sc1\n" \
  "s_waitcnt vmcnt(0)\n" \
  "s_branch 36b\n" \
  "37:\n" \
  "v_readfirstlane_b32 %[kk], v124\n" \
  "s_mov_b32 %[sob], 0x9c0000\n" \
  "s_set_gpr_idx_on %[pf], gpr_idx(SRC0)\n" \
  "s_nop 0\n" \
  "v_lshl_add_u32 v96, v144, 8, %[a0]\n" \
  "v_lshl_add_u32 v97, v158, 8, %[a1]\n" \
  "v_lshl_add_u32 v98, v172, 8, %[a2]\n" \
  "v_lshl_add_u32 v99, v186, 8, %[a3]\n" \
  "v_lshl_add_u32 v100, v200, 8, %[a0]\n" \
  "v_lshl_add_u32 v101, v214, 8, %[a1]\n" \
  "v_lshl_add_u32 v102, v228, 8, %[a2]\n" \
  "v_lshl_add_u32 v103, v242, 8, %[a3]\n" \
  "s_set_gpr_idx_off\n" \
  "s_add_u32 %[pf], %[pf], 1\n" \
  "s_cmp_eq_u32 %[pf], 14\n" \
  "s_cselect_b32 %[pf], 0, %[pf]\n" \
  "s_barrier\n" \
  "s_cmp_eq_u32 %[ka], 0\n" \
  "s_cbranch_scc1 51f\n" \
  "s_add_u32 m0, %[pk], 0x0\n" \
  "s_nop 0\n" \
  "buffer_load_dwordx4 v96, %[rsa], 0 offen lds\n" \
  "buffer_load_dwordx4 v97, %[rsa], 0 offen offset:1024 lds\n" \
  "buffer_load_dwordx4 v98, %[rsa], 0 offen offset:2048 lds\n" \
  "buffer_load_dwordx4 v99, %[rsa], 0 offen offset:3072 lds\n" \
  "s_add_u32 m0, m0, 0x1000\n" \
  "s_nop 0\n" \
  "buffer_load_dwordx4 v100, %[rsa], 0 offen lds\n" \
  "buffer_load_dwordx4 v101, %[rsa], 0 offen offset:1024 lds\n" \
  "buffer_load_dwordx4 v102, %[rsa], 0 offen offset:2048 lds\n" \
  "buffer_load_dwordx4 v103, %[rsa], 0 offen offset:3072 lds\n" \
  "s_branch 52f\n" \
  "51:\n" \
  "s_add_u32 m0, %[pk], 0x0\n" \
  "s_add_u32 %[slot], %[sob], %[kd]\n" \
  "buffer_load_dwordx4 %[b0], %[rsb], %[sob] offen lds\n" \
  "buffer_load_dwordx4 %[b1], %[rsb], %[sob] offen offset:1024 lds\n" \
  "buffer_load_dwordx4 %[b2], %[rsb], %[sob] offen offset:2048 lds\n" \
  "buffer_load_dwordx4 %[b3], %[rsb], %[sob] offen offset:3072 lds\n" \
  "s_xor_b32 m0, m0, 0x1000\n" \
  "s_nop 0\n" \
  "buffer_load_dwordx4 %[b0], %[rsb], %[slot] offen lds\n" \
  "buffer_load_dwordx4 %[b1], %[rsb], %[slot] offen offset:1024 lds\n" \
  "buffer_load_dwordx4 %[b2], %[rsb], %[slot] offen offset:2048 lds\n" \
  "buffer_load_dwordx4 %[b3], %[rsb], %[slot] offen offset:3072 lds\n" \
  "52:\n" \
  "s_add_u32 %[sob], %[sob], 0x68000\n" \
  "v_lshl_add_u32 v128, v128, 8, %[xc]\n" \
  "v_lshl_add_u32 v129, v129, 8, %[xc]\n" \
  "v_lshl_add_u32 v130, v130, 8, %[xc]\n" \
  "v_lshl_add_u32 v131, v131, 8, %[xc]\n" \
  "s_mov_b32 m0, %[xb]\n" \
  "s_nop 0\n" \
  "buffer_load_dwordx4 v128, %[rsa], %[xo] offen lds\n" \
  "s_add_u32 m0, m0, 0x400\n" \
  "s_nop 0\n" \
  "buffer_load_dwordx4 v129, %[rsa], %[xo] offen lds\n" \
  "s_add_u32 m0, m0, 0x400\n" \
  "s_nop 0\n" \
  "buffer_load_dwordx4 v130, %[rsa], %[xo] offen lds\n" \
  "s_add_u32 m0, m0, 0x400\n" \
  "s_mov_b64 exec, 0xffff\n" \
  "buffer_load_dwordx4 v131, %[rsa], %[xo] offen lds\n" \
  "s_mov_b64 exec, -1\n" \
  "v_readlane_b32 s84, v120, %[pf]\n" \
  "v_readlane_b32 s85, v121, %[pf]\n" \
  "v_readlane_b32 s86, v122, %[pf]\n" \
  "s_min_u32 s84, s84, 0x1869f\n" \
  "s_lshl_b32 s84, s84, 8\n" \
  "s_min_u32 s85, s85, 0x1869f\n" \
  "s_lshl_b32 s85, s85, 8\n" \
  "s_min_u32 s86, s86, 0x1869f\n" \
  "s_lshl_b32 s86, s86, 8\n" \
  "s_load_dword s87, %[tab], s84 offset:0\n" \
  "s_load_dword s87, %[tab], s84 offset:64\n" \
  "s_load_dword s87, %[tab], s85 offset:0\n" \
  "s_load_dword s87, %[tab], s85 offset:64\n" \
  "s_load_dword s87, %[tab], s86 offset:0\n" \
  "s_load_dword s87, %[tab], s86 offset:64\n" \
  "s_load_dword s87, %[tab], s84 offset:128\n" \
  "s_load_dword s87, %[tab], s84 offset:192\n" \
  "s_load_dword s87, %[tab], s85 offset:128\n" \
  "s_load_dword s87, %[tab], s85 offset:192\n" \
  "s_load_dword s87, %[tab], s86 offset:128\n" \
  "s_load_dword s87, %[tab], s86 offset:192\n" \
  "s_waitcnt vmcnt(12)\n" \
  "s_barrier\n" \
  "s_cmp_eq_u32 %[ka], 0\n" \
  "s_cbranch_scc1 61f\n" \
  "s_add_u32 m0, %[pk], 0x8000\n" \
  "s_nop 0\n" \
  "buffer_load_dwordx4 v96, %[rsa], %[h] offen lds\n" \
  "buffer_load_dwordx4 v97, %[rsa], %[h] offen offset:1024 lds\n" \
  "buffer_load_dwordx4 v98, %[rsa], %[h] offen offset:2048 lds\n" \
  "buffer_load_dwordx4 v99, %[rsa], %[h] offen offset:3072 lds\n" \
  "s_add_u32 m0, m0, 0x1000\n" \
  "s_nop 0\n" \
  "buffer_load_dwordx4 v100, %[rsa], %[h] offen lds\n" \
  "buffer_load_dwordx4 v101, %[rsa], %[h] offen offset:1024 lds\n" \
  "buffer_load_dwordx4 v102, %[rsa], %[h] offen offset:2048 lds\n" \
  "buffer_load_dwordx4 v103, %[rsa], %[h] offen offset:3072 lds\n" \
  "s_branch 62f\n" \
  "61:\n" \
  "s_add_u32 m0, %[pk], 0x8000\n" \
  "s_add_u32 %[slot], %[sob], %[kd]\n" \
  "buffer_load_dwordx4 %[b0], %[rsb], %[sob] offen lds\n" \
  "buffer_load_dwordx4 %[b1], %[rsb], %[sob] offen offset:1024 lds\n" \
  "buffer_load_dwordx4 %[b2], %[rsb], %[sob] offen offset:2048 lds\n" \
  "buffer_load_dwordx4 %[b3], %[rsb], %[sob] offen offset:3072 lds\n" \
  "s_xor_b32 m0, m0, 0x1000\n" \
  "s_nop 0\n" \
  "buffer_load_dwordx4 %[b0], %[rsb], %[slot] offen lds\n" \
  "buffer_load_dwordx4 %[b1], %[rsb], %[slot] offen offset:1024 lds\n" \
  "buffer_load_dwordx4 %[b2], %[rsb], %[slot] offen offset:2048 lds\n" \
  "buffer_load_dwordx4 %[b3], %[rsb], %[slot] offen offset:3072 lds\n" \
  "62:\n" \
  "s_add_u32 %[sob], %[sob], 0x68000\n" \
  "s_set_gpr_idx_on %[pf], gpr_idx(SRC0)\n" \
  "s_nop 0\n" \
  "v_lshl_add_u32 v96, v144, 8, %[a0]\n" \
  "v_lshl_add_u32 v97, v158, 8, %[a1]\n" \
  "v_lshl_add_u32 v98, v172, 8, %[a2]\n" \
  "v_lshl_add_u32 v99, v186, 8, %[a3]\n" \
  "v_lshl_add_u32 v100, v200, 8, %[a0]\n" \
  "v_lshl_add_u32 v101, v214, 8, %[a1]\n" \
  "v_lshl_add_u32 v102, v228, 8, %[a2]\n" \
  "v_lshl_add_u32 v103, v242, 8, %[a3]\n" \
  "s_set_gpr_idx_off\n" \
  "s_cmp_eq_u32 %[ka], 0\n" \
  "s_cbranch_scc1 71f\n" \
  "s_add_u32 m0, %[pk], 0x10000\n" \
  "s_nop 0\n" \
  "buffer_load_dwordx4 v96, %[rsa], 0 offen lds\n" \
  "buffer_load_dwordx4 v97, %[rsa], 0 offen offset:1024 lds\n" \
  "buffer_load_dwordx4 v98, %[rsa], 0 offen offset:2048 lds\n" \
  "buffer_load_dwordx4 v99, %[rsa], 0 offen offset:3072 lds\n" \
  "s_add_u32 m0, m0, 0x1000\n" \
  "s_nop 0\n" \
  "buffer_load_dwordx4 v100, %[rsa], 0 offen lds\n" \
  "buffer_load_dwordx4 v101, %[rsa], 0 offen offset:1024 lds\n" \
  "buffer_load_dwordx4 v102, %[rsa], 0 offen offset:2048 lds\n" \
  "buffer_load_dwordx4 v103, %[rsa], 0 offen offset:3072 lds\n" \
  "s_branch 72f\n" \
  "71:\n" \
  "s_add_u32 m0, %[pk], 0x10000\n" \
  "s_add_u32 %[slot], %[sob], %[kd]\n" \
  "buffer_load_dwordx4 %[b0], %[rsb], %[sob] offen lds\n" \
  "buffer_load_dwordx4 %[b1], %[rsb], %[sob] offen offset:1024 lds\n" \
  "buffer_load_dwordx4 %[b2], %[rsb], %[sob] offen offset:2048 lds\n" \
  "buffer_load_dwordx4 %[b3], %[rsb], %[sob] offen offset:3072 lds\n" \
  "s_xor_b32 m0, m0, 0x1000\n" \
  "s_nop 0\n" \
  "buffer_load_dwordx4 %[b0], %[rsb], %[slot] offen lds\n" \
  "buffer_load_dwordx4 %[b1], %[rsb], %[slot] offen offset:1024 lds\n" \
  "buffer_load_dwordx4 %[b2], %[rsb], %[slot] offen offset:2048 lds\n" \
  "buffer_load_dwordx4 %[b3], %[rsb], %[slot] offen offset:3072 lds\n" \
  "72:\n" \
  "s_add_u32 %[sob], %[sob], 0x68000\n" \
  "s_waitcnt vmcnt(8)\n" \
  "s_barrier\n" \
  "s_cmp_eq_u32 %[ka], 0\n" \
  "s_cbranch_scc1 81f\n" \
  "s_add_u32 m0, %[pk], 0x18000\n" \
  "s_nop 0\n" \
  "buffer_load_dwordx4 v96, %[rsa], %[h] offen lds\n" \
  "buffer_load_dwordx4 v97, %[rsa], %[h] offen offset:1024 lds\n" \
  "buffer_load_dwordx4 v98, %[rsa], %[h] offen offset:2048 lds\n" \
  "buffer_load_dwordx4 v99, %[rsa], %[h] offen offset:3072 lds\n" \
  "s_add_u32 m0, m0, 0x1000\n" \
  "s_nop 0\n" \
  "buffer_load_dwordx4 v100, %[rsa], %[h] offen lds\n" \
  "buffer_load_dwordx4 v101, %[rsa], %[h] offen offset:1024 lds\n" \
  "buffer_load_dwordx4 v102, %[rsa], %[h] offen offset:2048 lds\n" \
  "buffer_load_dwordx4 v103, %[rsa], %[h] offen offset:3072 lds\n" \
  "s_branch 82f\n" \
  "81:\n" \
  "s_add_u32 m0, %[pk], 0x18000\n" \
  "s_add_u32 %[slot], %[sob], %[kd]\n" \
  "buffer_load_dwordx4 %[b0], %[rsb], %[sob] offen lds\n" \
  "buffer_load_dwordx4 %[b1], %[rsb], %[sob] offen offset:1024 lds\n" \
  "buffer_load_dwordx4 %[b2], %[rsb], %[sob] offen offset:2048 lds\n" \
  "buffer_load_dwordx4 %[b3], %[rsb], %[sob] offen offset:3072 lds\n" \
  "s_xor_b32 m0, m0, 0x1000\n" \
  "s_nop 0\n" \
  "buffer_load_dwordx4 %[b0], %[rsb], %[slot] offen lds\n" \
  "buffer_load_dwordx4 %[b1], %[rsb], %[slot] offen offset:1024 lds\n" \
  "buffer_load_dwordx4 %[b2], %[rsb], %[slot] offen offset:2048 lds\n" \
  "buffer_load_dwordx4 %[b3], %[rsb], %[slot] offen offset:3072 lds\n" \
  "82:\n" \
  "s_add_u32 %[sob], %[sob], 0x68000\n" \
  "s_waitcnt lgkmcnt(0)\n"
#define GEMM_PROD_CLOB "s82", "s83", "s84", "s85", "s86", "s87", "v96", "v97", "v98", "v99", "v100", "v101", "v102", "v103", "v104", "v105", "v106", "v107", "v108", "v109", "v110", "v111", "v112", "v113", "v114", "v115", "v116", "v117", "v118", "v119", "v120", "v121", "v122", "v123", "v144", "v145", "v146", "v147", "v148", "v149", "v150", "v151", "v152", "v153", "v154", "v155", "v156", "v157", "v158", "v159", "v160", "v161", "v162", "v163", "v164", "v165", "v166", "v167", "v168", "v169", "v170", "v171", "v172", "v173", "v174", "v175", "v176", "v177", "v178", "v179", "v180", "v181", "v182", "v183", "v184", "v185", "v186", "v187", "v188", "v189", "v190", "v191", "v192", "v193", "v194", "v195", "v196", "v197", "v198", "v199", "v200", "v201", "v202", "v203", "v204", "v205", "v206", "v207", "v208", "v209", "v210", "v211", "v212", "v213", "v214", "v215", "v216", "v217", "v218", "v219", "v220", "v221", "v222", "v223", "v224", "v225", "v226", "v227", "v228", "v229", "v230", "v231", "v232", "v233", "v234", "v235", "v236", "v237", "v238", "v239", "v240", "v241", "v242", "v243", "v244", "v245", "v246", "v247", "v248", "v249", "v250", "v251", "v252", "v253", "v254", "v255"

namespace {
constexpr int NB = 1024, NF = 26, DIN = 3328, NV = 100000;
constexpr int ROWB = DIN * 2;
constexpr int SLOT = 49152, G_IDX = 3 * SLOT;
constexpr int XBUF = 3328;
constexpr int C_IDX = 0, C_XA = 2048, C_W2 = 41984, C_W3 = 77824, C_W1 = 113664, C_T = 113664, C_UT = 158720, C_MISC = 160768;
constexpr int C_WF = 41984, C_XB = 90624, C_DUMMY = C_MISC + 64, C_RES = C_MISC + 320, FP = 1056;
constexpr int LDS_BYTES = 163840, XEARLY = 12;
__device__ __forceinline__ unsigned xaddr(int u) { return (u < XEARLY ? C_XA : C_XB - XEARLY * XBUF) + u * XBUF; }
constexpr int WS_RDY = 1024;
constexpr int WS_GRAN = 8192;
constexpr int WS_X = 4 << 20;
#define XD_OUTF ""
#define XD_SL2 1
#define XD_LATE 60
#define XD_BT 1
#define PF_DEPTH 4
#define PF_LEAD 4
#define PF_NL 56
constexpr int NCW = 7;
constexpr int CSAMP = 608;
constexpr int WS_WFL = 7680, WS_W = 1 << 19;
constexpr int NBIG = 32;

__device__ const unsigned CIN_TAB[768] = CIN_TABLE_INIT;

__device__ __forceinline__ float bf_lo(unsigned v) { return __uint_as_float(v << 16); }
__device__ __forceinline__ float bf_hi(unsigned v) { return __uint_as_float(v & 0xffff0000u); }
__device__ __forceinline__ unsigned pack_bf16(float a, float b) { f32x2 p = {a, b}; return __builtin_bit_cast(unsigned, __builtin_convertvector(p, bf16x2)); }
__device__ __forceinline__ unsigned pack_f16(float a, float b) { f32x2 p = {a, b}; return __builtin_bit_cast(unsigned, __builtin_convertvector(p, f16x2)); }
__device__ __forceinline__ u32x4 rsrc4(const void* p, unsigned bytes) { const u64 a = (u64)p; u32x4 r = {(unsigned)a, (unsigned)(a >> 32) & 0xffffu, bytes, 0x00020000u}; return r; }
__device__ __forceinline__ int lane_id() { int l; asm volatile("v_mbcnt_lo_u32_b32 %0, -1, 0\n\tv_mbcnt_hi_u32_b32 %0, -1, %0" : "=v"(l)); return l; }
__device__ __forceinline__ unsigned opaque(unsigned x) { asm("" : "+v"(x)); return x; }
__device__ __forceinline__ void lds_max(unsigned a, unsigned v) { asm volatile("ds_max_u32 %0, %1" ::"v"(a), "v"(v) : "memory"); }
__device__ __forceinline__ float max3abs(float m, float a, float b) { float r; asm("v_max3_f32 %0, %1, |%2|, |%3|" : "=v"(r) : "v"(m), "v"(a), "v"(b)); return r; }
#define SCHED() __builtin_amdgcn_sched_barrier(0)
__device__ __forceinline__ void lbar(int n) {
  unsigned v;
  asm volatile("s_waitcnt lgkmcnt(0)\n\ts_mov_b64 exec, 1\n\tds_add_u32 %1, %2 offset:16\n\ts_mov_b64 exec, -1\n"
               "s_branch 2f\n1:\n\ts_sleep 1\n2:\n\tds_read_b32 %0, %1 offset:16\n\ts_waitcnt lgkmcnt(0)\n\tv_cmp_gt_u32 vcc, %3, %0\n\ts_cbranch_vccnz 1b"
               : "=&v"(v) : "v"(C_MISC), "v"(1), "s"(n * NCW) : "memory", "vcc");
}
template <int N> __device__ __forceinline__ void waitv() { asm volatile("s_waitcnt vmcnt(%0)" ::"i"(N) : "memory"); }
__device__ __forceinline__ void dma16(u32x4 rs, unsigned lds, unsigned voff, unsigned soff) {
  asm volatile("s_mov_b32 m0, %0\n\ts_nop 0\n\tbuffer_load_dwordx4 %1, %2, %3 offen lds" ::"s"(lds), "v"(voff), "s"(rs), "s"(soff) : "memory");
}
__device__ __forceinline__ void dma4(u32x4 rs, unsigned lds, unsigned voff, unsigned soff) {
  asm volatile("s_mov_b32 m0, %0\n\ts_nop 0\n\tbuffer_load_dword %1, %2, %3 offen lds" ::"s"(lds), "v"(voff), "s"(rs), "s"(soff) : "memory");
}
template <class T> __device__ __forceinline__ T ldsr(unsigned a) { return *(const LDS_AS T*)(size_t)a; }
template <class T> __device__ __forceinline__ void ldsw(unsigned a, T v) { *(LDS_AS T*)(size_t)a = v; }
__device__ __forceinline__ unsigned ldsv(unsigned a) { return *(const volatile LDS_AS unsigned*)(size_t)a; }
__device__ __forceinline__ void ldsvw(unsigned a, unsigned v) { *(volatile LDS_AS unsigned*)(size_t)a = v; }
__device__ __forceinline__ void publish(char* ws, int slot, int row, u64 tag, float v) {
  const u64 g = tag | (u64)(__float_as_uint(v) >> 8);
  __hip_atomic_store((GLB_AS u64*)(ws + WS_GRAN) + slot * 1024 + row, g, __ATOMIC_RELAXED, __HIP_MEMORY_SCOPE_AGENT);
}
__device__ __forceinline__ unsigned wave_max_u32(unsigned v) {
  v = max(v, (unsigned)__builtin_amdgcn_update_dpp(0, (int)v, 0x111, 0xf, 0xf, true));
  v = max(v, (unsigned)__builtin_amdgcn_update_dpp(0, (int)v, 0x112, 0xf, 0xf, true));
  v = max(v, (unsigned)__builtin_amdgcn_update_dpp(0, (int)v, 0x114, 0xf, 0xf, true));
  v = max(v, (unsigned)__builtin_amdgcn_update_dpp(0, (int)v, 0x118, 0xf, 0xf, true));
  return v;
}
__device__ __forceinline__ void xload(u32x4 rsA, int ubase, int u, unsigned buf, int lane, unsigned xch) {
  const int gu = ubase + u;
  const unsigned ia = C_IDX + ((gu >> 1) - (ubase >> 1)) * (NF * 4) + (lane >> 3) * 4;
  const int i0 = ldsr<int>(ia), i1 = ldsr<int>(ia + 32), i2 = ldsr<int>(ia + 64), i3 = ldsr<int>(ia + 96);
  const unsigned so = (gu & 1) * 128;
  dma16(rsA, buf, i0 * 256 + xch, so); dma16(rsA, buf + 1024, i1 * 256 + xch, so); dma16(rsA, buf + 2048, i2 * 256 + xch, so);
  if (lane < 16) dma16(rsA, buf + 3072, i3 * 256 + xch, so);
}
template <int RB> __device__ __forceinline__ void final_sum(char* ws, u16* out, const u16* bo, u64 tag, int row0, int n, unsigned res, unsigned cnt, unsigned need) {
  constexpr int R = 1 << RB, G = 64 >> RB, NL = (52 + G - 1) / G;
  const int lane = lane_id(), r = min(lane & (R - 1), n - 1), sg = lane >> RB;
  const GLB_AS u64* gp = (const GLB_AS u64*)(ws + WS_GRAN) + sg * 1024 + row0 + r;
  float bias = bf_lo(bo[0]);
  asm volatile("" : "+v"(bias));
  float s; bool ok;
  if constexpr (NL == 2) {
    const GLB_AS u64* gq = gp + (sg < 52 - G ? G * 1024 : 0);
    unsigned n = 1 << 16, t0, t1;
    asm volatile("global_load_dwordx2 v[24:25], %[p], off sc1\n\tglobal_load_dwordx2 v[26:27], %[q], off sc1\n\ts_sleep 3\n"
                 "1:\n\tglobal_load_dwordx2 v[28:29], %[p], off sc1\n\tglobal_load_dwordx2 v[30:31], %[q], off sc1\n\ts_waitcnt vmcnt(2)\n\t"
                 "v_xor_b32 %[t0], %[lo], v24\n\tv_xor_b32 %[t1], %[lo], v26\n\tv_or_b32 %[t0], %[t0], %[t1]\n\tv_lshrrev_b32 %[t0], 24, %[t0]\n\t"
                 "v_xor_b32 %[t1], %[hi], v25\n\tv_or_b32 %[t0], %[t0], %[t1]\n\tv_xor_b32 %[t1], %[hi], v27\n\tv_or_b32 %[t0], %[t0], %[t1]\n\t"
                 "v_cmp_ne_u32 vcc, 0, %[t0]\n\ts_sub_u32 %[n], %[n], 1\n\ts_cbranch_vccz 3f\n\ts_cbranch_scc1 2f\n\t"
                 "global_load_dwordx2 v[24:25], %[p], off sc1\n\tglobal_load_dwordx2 v[26:27], %[q], off sc1\n\ts_waitcnt vmcnt(2)\n\t"
                 "v_xor_b32 %[t0], %[lo], v28\n\tv_xor_b32 %[t1], %[lo], v30\n\tv_or_b32 %[t0], %[t0], %[t1]\n\tv_lshrrev_b32 %[t0], 24, %[t0]\n\t"
                 "v_xor_b32 %[t1], %[hi], v29\n\tv_or_b32 %[t0], %[t0], %[t1]\n\tv_xor_b32 %[t1], %[hi], v31\n\tv_or_b32 %[t0], %[t0], %[t1]\n\t"
                 "v_cmp_ne_u32 vcc, 0, %[t0]\n\ts_nop 0\n\ts_cbranch_vccnz 1b\n\t"
                 "v_lshlrev_b32 %[t0], 8, v28\n\tv_lshlrev_b32 %[t1], 8, v30\n\ts_branch 4f\n"
                 "2:\n\tv_mov_b32 %[b], 0x7fc00000\n"
                 "3:\n\tv_lshlrev_b32 %[t0], 8, v24\n\tv_lshlrev_b32 %[t1], 8, v26\n"
                 "4:\n\tv_and_b32 %[t1], %[m], %[t1]\n\tv_add_f32 %[t0], %[t0], %[t1]\n\ts_nop 1\n\t"
                 "v_add_f32_dpp %[t0], %[t0], %[t0] row_ror:2\n\ts_nop 1\n\tv_add_f32_dpp %[t0], %[t0], %[t0] row_ror:4\n\ts_nop 1\n\t"
                 "v_add_f32_dpp %[t0], %[t0], %[t0] row_ror:8\n\tv_mov_b32 %[t1], %[t0]\n\ts_nop 1\n\tv_permlane16_swap_b32 %[t0], %[t1]\n\t"
                 "v_add_f32 %[t0], %[t0], %[t1]\n\tv_mov_b32 %[t1], %[t0]\n\ts_nop 1\n\tv_permlane32_swap_b32 %[t0], %[t1]\n\t"
                 "v_add_f32 %[t0], %[t0], %[t1]\n\ts_mov_b32 %[n], 0x100000\n"
                 "5:\n\tds_read_b32 %[t1], %[c]\n\tds_read_b64 v[32:33], %[r]\n\ts_sub_u32 %[n], %[n], 1\n\ts_waitcnt lgkmcnt(0)\n\t"
                 "v_cmp_gt_u32 vcc, %[need], %[t1]\n\ts_cbranch_scc1 6f\n\ts_cbranch_vccz 7f\n\ts_sleep 1\n\ts_branch 5b\n"
                 "6:\n\tv_mov_b32 %[b], 0x7fc00000\n"
                 "7:\n\tv_add_f32 v32, v32, v33\n\tv_add_f32 %[t0], %[t0], v32\n\tv_add_f32 %[t0], %[t0], %[b]\n\tv_cvt_pk_bf16_f32 %[t0], %[t0], %[t0]\n\t"
                 "s_mov_b64 exec, 3\n\tglobal_store_short %[o], %[t0], %[out]\n\ts_mov_b64 exec, -1\n\ts_waitcnt vmcnt(0)"
                 : [t0] "=&v"(t0), [t1] "=&v"(t1), [n] "+s"(n), [b] "+v"(bias)
                 : [p] "v"(gp), [q] "v"(gq), [lo] "s"((unsigned)tag), [hi] "s"((unsigned)(tag >> 32)), [m] "v"(sg < 52 - G ? ~0u : 0u), [c] "v"(cnt),
                   [r] "v"(res + r * 8), [need] "s"(need), [o] "v"((row0 + r) * 2), [out] "s"(out)
                 : "memory", "vcc", "scc", "v24", "v25", "v26", "v27", "v28", "v29", "v30", "v31", "v32", "v33");
    return;
  } else {
    for (int spin = 0;; ++spin) {
      u64 g[NL];
#pragma unroll
      for (int i = 0; i < NL; ++i) g[i] = __hip_atomic_load(gp + i * G * 1024, __ATOMIC_RELAXED, __HIP_MEMORY_SCOPE_AGENT);
      ok = true; s = 0.f;
#pragma unroll
      for (int i = 0; i < NL; ++i) { ok &= ((g[i] ^ tag) >> 24) == 0; s += __uint_as_float((unsigned)g[i] << 8); }
      ok = __all(ok);
      if (ok || spin > (1 << 17)) break;
    }
  }
  {
    const auto x = __builtin_amdgcn_permlane16_swap(__float_as_uint(s), __float_as_uint(s), false, false);
    s = __uint_as_float(x[0]) + __uint_as_float(x[1]);
    const auto y = __builtin_amdgcn_permlane32_swap(__float_as_uint(s), __float_as_uint(s), false, false);
    s = __uint_as_float(y[0]) + __uint_as_float(y[1]);
  }
  for (int spin = 0; (unsigned)__builtin_amdgcn_readfirstlane(ldsv(cnt)) < need; ++spin) { if (spin > (1 << 20)) { ok = false; break; } __builtin_amdgcn_s_sleep(XD_SL2); }
  const f32x2 c = ldsr<f32x2>(res + r * 8);
  const unsigned o = ok ? pack_bf16(s + (c.x + c.y) + bias, 0.f) : 0x7fc0u;
  if (lane < n) asm volatile("global_store_short %0, %1, %2 " XD_OUTF ::"v"((row0 + lane) * 2), "v"(o), "s"(out) : "memory");
}
}

__global__ __launch_bounds__(512, 2) void xdeepfm_kernel(const int* __restrict__ idx, const u16* __restrict__ table, const u16* __restrict__ wc,
                                                         const u16* __restrict__ wco, const u16* __restrict__ w1, const u16* __restrict__ b1,
                                                         const u16* __restrict__ w2, const u16* __restrict__ bo, u16* __restrict__ out, char* __restrict__ ws) {
#if defined(__HIP_DEVICE_COMPILE__)
  const int tid = threadIdx.x, lane = tid & 63, w = __builtin_amdgcn_readfirstlane(tid >> 6);
  const int bid = blockIdx.x, xcd = bid & 7, j = bid >> 3;
  const int hh = lane >> 5, kh = xcd & 1, ng = xcd >> 1;
  const u32x4 rsI = rsrc4(idx, NB * NF * 4);
  const u32x4 rsA = rsrc4((const char*)table - 4096, NV * 256 + 4096);
  const u32x4 rsB = rsrc4((const char*)w1 - 4096, DIN * ROWB + 4096);
  u64 ep, tag;
#define EP_GET(more) asm volatile("s_load_dwordx2 %0, %1, 0\n\ts_waitcnt lgkmcnt(0)" more : "=&s"(ep) : "s"(ws) : "memory"); tag = (ep + 1) << 24
  const unsigned vI = (lane >> 2) * (NF * 4) + (lane & 3) * 16 + kh * 48;
  const int t = ng * 26 + (j - 6), tn = t >> 2, tm = t & 3;
  const u32x4 rsW = rsrc4(wc, 3 * 17576 * 2);
  auto wload = [&](unsigned dst, unsigned src, unsigned l16) {
    int p = j * 6 + w;
#pragma unroll
    for (int i = 0; i < 5; ++i) {
      p = p >= 35 ? p - 35 : p;
      dma16(rsW, dst + p * 1024, l16, src + p * 1024);
      p += NCW;
    }
  };
  if (__builtin_expect(j >= 6 && w >= 4, 1)) {
    const int pw = w - 4;
    asm volatile("s_mov_b32 m0, %0\n\ts_nop 0\n\tbuffer_load_dwordx4 %1, %5, %6 offen lds\n\tbuffer_load_dwordx4 %2, %5, %6 offen offset:1024 lds\n\t"
                 "buffer_load_dwordx4 %3, %5, %6 offen offset:2048 lds\n\tbuffer_load_dwordx4 %4, %5, %6 offen offset:3072 lds"
                 ::"s"(G_IDX + pw * 4096), "v"(vI), "v"(vI + (1664 - 1024)), "v"(vI + 2 * (1664 - 1024)), "v"(vI + 3 * (1664 - 1024)), "s"(rsI),
                   "s"((tm * 256 + pw * 64) * (NF * 4)) : "memory");
    unsigned vb[4];
#pragma unroll
    for (int i = 0; i < 4; ++i) vb[i] = 4096 - 1024 * i + ((pw * 4 + i) * 4 + (lane >> 4)) * ROWB + tn * 256 + ((lane & 15) ^ (((lane >> 4) << 2) | i)) * 16;
    asm volatile("s_mov_b32 m0, %0\n\ts_nop 0\n\tbuffer_load_dwordx4 %1, %5, %6 offen lds\n\tbuffer_load_dwordx4 %2, %5, %6 offen offset:1024 lds\n\t"
                 "buffer_load_dwordx4 %3, %5, %6 offen offset:2048 lds\n\tbuffer_load_dwordx4 %4, %5, %6 offen offset:3072 lds"
                 ::"s"(32768 + pw * 4096), "v"(vb[0]), "v"(vb[1]), "v"(vb[2]), "v"(vb[3]), "s"(rsB), "s"(kh * (28 * 64 * ROWB)) : "memory");
  }
  asm volatile("" ::: "memory");
  const int cw = xcd * 6 + j;
  int ubase = j >= 6 ? 2 * CSAMP + (kh * 104 + t) * 4 + (w - 4) : 2 * (cw * 13 - max(cw - NBIG, 0));
  if (j >= 6) {
  } else if (w < NCW) {
    dma16(rsI, C_IDX + (w & 1) * 1024, lane * 16, (ubase >> 1) * (NF * 4) + (w & 1) * 1024);
    wload(C_W1, 0, lane * 16);
    wload(C_W3, 70304, lane * 16);
  } else {
    u64 pc; unsigned len;
    asm volatile("s_getpc_b64 %0\n.Lxd_here:\n\ts_mov_b32 %1, .Lxd_end-.Lxd_here" : "=s"(pc), "=s"(len));
    dma4(rsrc4((const void*)pc, len), C_DUMMY, lane * 128, j * 8192);
  }
  int role = __builtin_amdgcn_readfirstlane(j >= 6 ? (w >= 4 ? 0 : 1) : (w < NCW ? 2 : 3));
  asm volatile("" : "+s"(role));
  u32x8 a1q;
  unsigned cur = 0, wl, off = 1;
  int u = 0, ucnt = 1, k1, k2;
  if (role < 2) {
    if (role == 0) {
      const int lane = lane_id(), hh = lane >> 5, tid = w * 64 + lane; const unsigned xch = 4096 + (lane & 7) * 16; (void)hh; (void)tid; (void)xch;
      const int pw = w - 4;
      unsigned vb[4], ca[4];
#pragma unroll
      for (int i = 0; i < 4; ++i) vb[i] = 4096 - 1024 * i + ((pw * 4 + i) * 4 + (lane >> 4)) * ROWB + tn * 256 + ((lane & 15) ^ (((lane >> 4) << 2) | i)) * 16;
#pragma unroll
      for (int i = 0; i < 4; ++i) ca[i] = 4096 - 1024 * i + (((lane & 7) ^ ((i & 1) * 4 + (lane >> 4))) * 16);
      const unsigned pa = pw * 8192, pb = 32768 + pw * 4096;
      unsigned sob = kh * (28 * 64 * ROWB), slot, rem = 11;
      const unsigned ra = G_IDX + (pw * 64 + (lane >> 3)) * 64;
      unsigned pf = 2 * kh;
      const int ka = ((pw ^ kh) & 1) ^ 1;
      cur = G_IDX + pw * 4096;
      off = G_IDX + 3328;
      const unsigned p0 = G_IDX + (pw * 64 + tn) * 64 + (lane & 15) * 4;
      const unsigned tv = (XD_BT && lane < 8 * XD_BT) ? 4096 + (64 + (lane >> 3) * 64 + tm * 16 + pw * 4 + ((lane >> 1) & 3)) * ROWB + tn * 256 + (lane & 1) * 128 : 0x80000000u;
      u64 fl = 0;
      const int me = (t * 2 + kh) * 4 + pw;
      const unsigned fa = lane < 46 ? WS_WFL + lane * 8 : WS_RDY + (me ^ 4) * 8;
      unsigned kk;
      asm volatile("s_load_dwordx2 s[88:89], %[rme], 0\n\ts_waitcnt vmcnt(4) lgkmcnt(0)\n\ts_add_u32 s84, s88, 1\n\ts_addc_u32 s85, s89, 0\n\ts_lshl_b64 s[84:85], s[84:85], 24\n\t"
                   "v_mov_b32 v126, s84\n\tv_mov_b32 v127, s85\n" GEMM_PROD_ASM
                   : [slot] "=&s"(slot), [sob] "+s"(sob), [rem] "+s"(rem), "+{v[124:125]}"(fl), "=&{v[132:139]}"(a1q),
                     [pf] "+s"(pf), [kk] "=&s"(kk), "=&{s[88:89]}"(ep)
                   : [ra] "v"(ra), [b0] "v"(vb[0]), [b1] "v"(vb[1]), [b2] "v"(vb[2]), [b3] "v"(vb[3]), [a0] "v"(ca[0]), [a1] "v"(ca[1]), [a2] "v"(ca[2]),
                     [a3] "v"(ca[3]), [rsa] "s"(rsA), [rsb] "s"(rsB), [pa] "s"(pa), [pb] "s"(pb), [h] "s"(128), [tab] "s"(table),
                     [p0] "v"(p0), [p2] "v"(p0 + (tn < 12 ? 52 * 64 : 0)),
                     [xa] "v"((lane >> 3) * 4), [wa] "v"(hh * 8), [rsi] "s"(rsI), [xs] "s"((ubase >> 1) * (NF * 4)), [wco] "s"(wco),
                     [rsx] "s"(rsrc4(ws + WS_X, 208 * 32768)), [sme] "s"(me * 8192), [rme] "s"(ws), [zv] "v"(WS_RDY + me * 8), [zc] "v"(cur + 3328), [l16] "v"(lane * 16), [tv] "v"(tv),
                     [r0] "v"(ra + kh * 8), [ka] "s"(ka), [pk] "s"(ka ? (pw >> 1) * 8192 : 16384 + pw * 4096), [kd] "s"(((pw ^ 1) - pw) * 16 * ROWB),
                     [xc] "v"(xch), [xb] "s"(cur), [xo] "s"((ubase & 1) * 128), "{v140}"(fa)
                   : "memory", "scc", "vcc", "v126", "v127", "v128", "v129", "v130", "v131", "v143", GEMM_PROD_CLOB);
      tag = (ep + 1) << 24;
      k1 = (int)((kk >> 12) & 0xfff) - 2048; k2 = (int)(kk & 0xfff) - 2048;
      wl = lane * 16;
    } else {
      const int lane = lane_id(), hh = lane >> 5, tid = w * 64 + lane; const unsigned xch = 4096 + (lane & 7) * 16; (void)hh; (void)tid; (void)xch;
      const int wm = w >> 1, wn = w & 1, g4 = lane >> 4;
      const int n0 = tn * 128 + wn * 64 + 4 * g4;
      const int me = (t * 2 + kh) * 4 + w, pr = me ^ 4;
      u32x2 bq[4], wq[4];
#pragma unroll
      for (int i = 0; i < 4; ++i) { bq[i] = *(const u32x2*)(b1 + n0 + 16 * i); wq[i] = *(const u32x2*)(w2 + n0 + 16 * i); }
      f32x16 c[8];
#pragma unroll
      for (int i = 0; i < 4; ++i) { c[0][4 * i] = bf_lo(bq[i].x); c[0][4 * i + 1] = bf_hi(bq[i].x); c[0][4 * i + 2] = bf_lo(bq[i].y); c[0][4 * i + 3] = bf_hi(bq[i].y); }
      c[1] = c[0]; c[2] = c[0]; c[3] = c[0]; c[4] = f32x16{}; c[5] = f32x16{}; c[6] = f32x16{}; c[7] = f32x16{};
      const int q = (lane & 15) >> 2, p = lane & 3;
      u32x16 ad;
#pragma unroll
      for (int nt = 0; nt < 4; ++nt)
#pragma unroll
        for (int tt = 0; tt < 2; ++tt) ad[nt * 2 + tt] = 32768 + 256 * (8 * g4 + 4 * tt + q) + 16 * ((2 * (4 * wn + nt) + (p >> 1)) ^ ((q << 2) | ((2 * g4 + tt) & 3))) + 8 * (p & 1);
#pragma unroll
      for (int s = 0; s < 2; ++s)
#pragma unroll
        for (int h = 0; h < 2; ++h) ad[8 + s * 2 + h] = (wm * 128 + (h ^ kh) * 64 + (lane & 15)) * 128 + 16 * ((4 * s + g4) ^ ((lane & 15) >> 1));
      ad[12] = 0; ad[13] = 0; ad[14] = 0; ad[15] = 0;
      unsigned cnt = 23, ph = 0, dl;
      float res;
      const unsigned one = (q == g4) ? (0x3f80u << (16 * (lane & 1))) : 0u;
      const u32x8 cs = {(unsigned)lane * 16, (unsigned)lane * 16 + 4096, 0u, 0u, (lane & 2) ? 0u : one, (lane & 2) ? one : 0u, 0u, 0u};
      u32x8 wp = {wq[0].x, wq[0].y, wq[1].x, wq[1].y, wq[2].x, wq[2].y, wq[3].x, wq[3].y};
      EP_GET("");
      asm volatile(GEMM_CONS_ASM
                   : "+{v[64:79]}"(c[0]), "+{v[80:95]}"(c[1]), "+{v[96:111]}"(c[2]), "+{v[112:127]}"(c[3]), "+{v[128:143]}"(c[4]), "+{v[144:159]}"(c[5]),
                     "+{v[160:175]}"(c[6]), "+{v[176:191]}"(c[7]), "+{v[48:63]}"(ad), "+{v[32:39]}"(wp), [cnt] "+s"(cnt), [ph] "+s"(ph), [dl] "=&s"(dl), "=&{v192}"(res)
                   : "{v[40:47]}"(cs), [rsx] "s"(rsrc4(ws + WS_X, 208 * 32768)), [spr] "s"(pr * 8192), [sme] "s"(me * 8192),
                     [dx] "s"(-2 * SLOT - (wm + kh) * 8192)
                   : "memory", "scc", "vcc", GEMM_CONS_CLOB);
      {
      publish(ws, tn * 2 + wn, tm * 256 + wm * 128 + kh * 64 + lane_id(), tag, res);
      __builtin_amdgcn_s_setprio(0);
      if (w == 0) { final_sum<1>(ws, out, bo, tag, CSAMP + (kh * 104 + t) * 2, 2, G_IDX + 3336, G_IDX + 3328, 8); }
      }
      __builtin_amdgcn_endpgm();
    }
  } else {
    const int lane = lane_id(), hh = lane >> 5, tid = w * 64 + lane; const unsigned xch = 4096 + (lane & 7) * 16; (void)hh; (void)tid; (void)xch;
    if (role == 3) {
      const int l0 = 832 * (2 + XD_BT) + j * PF_NL + lane;
      int col = l0 % 13;
      unsigned vP = lane < PF_NL ? 4096 + (l0 / 13) * ROWB + (13 * ng + col) * 128 : 0x80000000u;
      const unsigned kb = kh * (28 * 64 * ROWB), kw = kh * (26 * 64 * ROWB);
      const u64 ts = __builtin_amdgcn_s_memtime();
      __builtin_amdgcn_s_setprio(3);
      EP_GET("\n\ts_barrier");
      int qok = 0;
#pragma nounroll
      for (int q = 832 * (2 + XD_BT) + j * PF_NL; q < 832 * 26; q += 6 * PF_NL) {
        while (q > qok) { qok = (unsigned)(__builtin_amdgcn_s_memtime() - ts) + PF_LEAD * 832; if (q > qok) __builtin_amdgcn_s_sleep(2); }
        dma4(rsB, C_DUMMY, vP, q + PF_NL > 832 * 24 ? kb - kw : kb);
        col += 6 * PF_NL % 13;
        vP += 6 * PF_NL / 13 * ROWB + 6 * PF_NL % 13 * 128 + (col >= 13 ? ROWB - 13 * 128 : 0);
        col -= col >= 13 ? 13 : 0;
        waitv<PF_DEPTH - 1>();
      }
      waitv<0>();
      ucnt = cw < NBIG ? 26 : 24;
      while ((unsigned)__builtin_amdgcn_readfirstlane(ldsv(C_MISC + 20)) < ucnt - 7) __builtin_amdgcn_s_sleep(32);
      final_sum<4>(ws, out, bo, tag, ubase >> 1, ucnt >> 1, C_RES, C_MISC + 20, ucnt);
      __builtin_amdgcn_endpgm();
    }
    ucnt = cw < NBIG ? 26 : 24;
    if (tid < 128) ldsw<u32x4>(C_UT + tid * 16, u32x4{0, 0, 0, 0});
    if (tid < 16) ldsw<unsigned>(C_MISC + tid * 4, tid == 0 ? NCW : 0);
    const int ti = opaque(tid), ig = ti >= 256 ? 1 : 0, it = max(ti - 64, 0) - ig * 192;
    float fa[13][2] = {}, fb[13][2] = {};
#define PIN26(f) asm volatile("" : "+v"(f[0][0]), "+v"(f[0][1]), "+v"(f[1][0]), "+v"(f[1][1]), "+v"(f[2][0]), "+v"(f[2][1]), "+v"(f[3][0]), "+v"(f[3][1]), "+v"(f[4][0]), \
    "+v"(f[4][1]), "+v"(f[5][0]), "+v"(f[5][1]), "+v"(f[6][0]), "+v"(f[6][1]), "+v"(f[7][0]), "+v"(f[7][1]), "+v"(f[8][0]), "+v"(f[8][1]), "+v"(f[9][0]), "+v"(f[9][1]), \
    "+v"(f[10][0]), "+v"(f[10][1]), "+v"(f[11][0]), "+v"(f[11][1]), "+v"(f[12][0]), "+v"(f[12][1]))
    PIN26(fa); PIN26(fb);
    u32x2 ta, tb, aq0, aq1, aq2, aq3; u32x8 a3a; u32x4 a3b; unsigned a2v, a3c, pp = j * 6 + w;
    asm volatile("global_load_dwordx2 %0, %12, %14\n\tglobal_load_dwordx2 %1, %12, %14 offset:1536\n\tglobal_load_dwordx2 %2, %13, %15\n\t"
                 "global_load_dwordx2 %3, %13, %15 offset:16\n\tglobal_load_dwordx2 %4, %13, %15 offset:32\n\tglobal_load_dwordx2 %5, %13, %15 offset:48\n\t"
                 "global_load_ushort %6, %16, %15 offset:52\n\ts_waitcnt lgkmcnt(0)\n\t"
                 "s_load_dwordx8 %7, %15, 0x68\n\ts_load_dwordx4 %8, %15, 0x88\n\ts_load_dword %9, %15, 0x98\n\ts_load_dwordx2 %10, %17, 0\n\t"
                 ".rept 5\n\ts_sub_u32 vcc_lo, %11, 35\n\ts_cselect_b32 %11, %11, vcc_lo\n\ts_lshl_b32 vcc_lo, %11, 10\n\ts_add_u32 m0, vcc_lo, %c20\n\t"
                 "s_add_u32 vcc_lo, vcc_lo, 35152\n\tbuffer_load_dwordx4 %18, %19, vcc_lo offen lds\n\ts_add_u32 %11, %11, 7\n\t.endr\n\t"
                 "s_barrier\n\ts_waitcnt vmcnt(5) lgkmcnt(0)"
                 : "=&v"(ta), "=&v"(tb), "=&v"(aq0), "=&v"(aq1), "=&v"(aq2), "=&v"(aq3), "=&v"(a2v), "=&s"(a3a), "=&s"(a3b), "=&s"(a3c), "=&s"(ep), "+s"(pp)
                 : "v"(it * 8), "v"(hh * 8), "s"(CIN_TAB), "s"(wco), "v"(min(lane, 25) * 2), "s"(ws), "v"(lane * 16), "s"(rsW), "i"(C_W2)
                 : "memory", "vcc", "scc");
    a1q = u32x8{aq0.x, aq0.y, aq1.x, aq1.y, aq2.x, aq2.y, aq3.x, aq3.y};
    tag = (ep + 1) << 24;
    lbar(1);
    u = w; off = 0;
    float mx = 0.f;
    {
      const unsigned base = C_W1 + ig * (13 * 1352);
      const unsigned g0 = base + (ta.x & 0xffff), g1 = base + ((ta.x >> 16) & 0x7fff), g2 = base + (ta.y & 0xffff), g3 = base + ((ta.y >> 16) & 0x7fff);
#define GA(i) asm volatile("ds_read_u16_d16_hi %0, %4 offset:%c8\n\tds_read_u16_d16_hi %1, %5 offset:%c8\n\tds_read_u16_d16_hi %2, %6 offset:%c8\n\tds_read_u16_d16_hi %3, %7 offset:%c8" \
                           : "+v"(fa[i][0]), "+v"(fb[i][0]), "+v"(fa[i][1]), "+v"(fb[i][1]) : "v"(g0), "v"(g1), "v"(g2), "v"(g3), "i"((i) * 1352) : "memory");
      GA(0) GA(1) GA(2) GA(3) GA(4) GA(5) GA(6) GA(7) GA(8) GA(9) GA(10) GA(11) GA(12)
#undef GA
      const float m0 = (int)ta.x < 0 ? 0.f : 1.f, m1 = (int)ta.y < 0 ? 0.f : 1.f;
      SCHED();
      asm volatile("s_waitcnt lgkmcnt(0)" ::: "memory");
      PIN26(fa); PIN26(fb);
#pragma unroll
      for (int i = 0; i < 13; ++i) {
        fa[i][0] = __builtin_fmaf(fb[i][0], m0, fa[i][0]); fa[i][1] = __builtin_fmaf(fb[i][1], m1, fa[i][1]);
        mx = max3abs(mx, fa[i][0], fa[i][1]);
      }
      const unsigned m = wave_max_u32(__float_as_uint(mx));
      if ((lane & 15) == 15) lds_max(C_MISC + 4, m);
    }
    lbar(2);
    k1 = min(15 - __builtin_amdgcn_readfirstlane(__builtin_amdgcn_frexp_expf(ldsr<float>(C_MISC + 4))), 120);
    const float sc1 = __uint_as_float((127 + k1) << 23);
    unsigned fw[13];
    const int q4 = (lane & 15) >> 2;
    unsigned trow[4];
#pragma unroll
    for (int r = 0; r < 4; ++r) trow[r] = min(16 * (r >> 1) + 8 * hh + 4 * (r & 1) + q4, 25) * 1352;
    {
      const unsigned col = C_W3 + (w * 32 + ((lane >> 4) & 1) * 16 + (lane & 3) * 4) * 2;
      s16x4 xa[4][4];
#pragma unroll
      for (int r = 0; r < 4; ++r) {
        const unsigned ra = opaque(col + trow[r]);
#pragma unroll
        for (int i = 0; i < 4; ++i) xa[i][r] = __builtin_amdgcn_ds_read_tr16_b64_v4i16((LDS_AS s16x4*)(size_t)(ra + i * (NCW * 64)));
      }
      const bf16x8 a0 = __builtin_bit_cast(bf16x8, hh ? u32x4{a3a[4], a3a[5], a3a[6], a3a[7]} : u32x4{a3a[0], a3a[1], a3a[2], a3a[3]});
      const bf16x8 a1 = __builtin_bit_cast(bf16x8, hh ? u32x4{a3c, 0u, 0u, 0u} : a3b);
      const f32x2 s1 = {sc1, sc1};
#pragma unroll
      for (int i = 0; i < 13; ++i) { const f32x2 v = f32x2{fa[i][0], fa[i][1]} * s1; fw[i] = pack_f16(v.x, v.y); }
      SCHED();
#pragma unroll
      for (int i = 0; i < 4; ++i) {
        if (i < 3 || w == 0) {
          f32x16 c = {};
          c = __builtin_amdgcn_mfma_f32_32x32x16_bf16(a0, __builtin_bit_cast(bf16x8, __builtin_shufflevector(xa[i][0], xa[i][1], 0, 1, 2, 3, 4, 5, 6, 7)), c, 0, 0, 0);
          c = __builtin_amdgcn_mfma_f32_32x32x16_bf16(a1, __builtin_bit_cast(bf16x8, __builtin_shufflevector(xa[i][2], xa[i][3], 0, 1, 2, 3, 4, 5, 6, 7)), c, 0, 0, 0);
          const unsigned hk = (w + NCW * i) * 32 + (lane & 31), h = (hk * 2521u) >> 16, k = hk - 26 * h;
          if (hk < 676) ldsw<u16>(C_UT + k * 64 + h * 2, (u16)pack_bf16(c[0], 0.f));
        }
      }
      if (w == 6 && lane < 26) ldsw<u16>(C_UT + 26 * 64 + lane * 2, (u16)a2v);
    }
    waitv<0>();
    xload(rsA, ubase, w, xaddr(w), lane, xch);
    lbar(3);
    f32x16 c[4];
    const int oi = (lane & 31) >> 2, oj = 4 * w + (lane & 3) - 2 * (oi & 1);
    const bool jok = (unsigned)oj < 26u;
    {
      const bf16x8 ub0 = ldsr<bf16x8>(C_UT + (lane & 31) * 64 + hh * 16), ub1 = ldsr<bf16x8>(C_UT + (lane & 31) * 64 + 32 + hh * 16);
      const int pi = ((lane >> 4) & 1) * 4 + (lane & 3);
      const unsigned col = C_W2 + (pi * 26 + 4 * w - 2 * (pi & 1)) * 2;
      s16x4 xa[4][4];
#pragma unroll
      for (int r = 0; r < 4; ++r) {
        const unsigned ra = opaque(col + trow[r]);
#pragma unroll
        for (int i = 0; i < 4; ++i) xa[i][r] = __builtin_amdgcn_ds_read_tr16_b64_v4i16((LDS_AS s16x4*)(size_t)(ra + i * 416));
      }
      SCHED();
#pragma unroll
      for (int i = 0; i < 4; ++i) {
        const f32x16 z = {};
        c[i] = __builtin_amdgcn_mfma_f32_32x32x16_bf16(ub0, __builtin_bit_cast(bf16x8, __builtin_shufflevector(xa[i][0], xa[i][1], 0, 1, 2, 3, 4, 5, 6, 7)), z, 0, 0, 0);
        c[i] = __builtin_amdgcn_mfma_f32_32x32x16_bf16(ub1, __builtin_bit_cast(bf16x8, __builtin_shufflevector(xa[i][2], xa[i][3], 0, 1, 2, 3, 4, 5, 6, 7)), c[i], 0, 0, 0);
      }
      float ma = 0.f, mb = 0.f;
#pragma unroll
      for (int r = 0; r < 14; r += 2) {
        ma = __builtin_fmaxf(__builtin_fmaxf(ma, __builtin_fabsf(c[0][r])), __builtin_fabsf(c[0][r + 1]));
        ma = __builtin_fmaxf(__builtin_fmaxf(ma, __builtin_fabsf(c[1][r])), __builtin_fabsf(c[1][r + 1]));
        ma = __builtin_fmaxf(__builtin_fmaxf(ma, __builtin_fabsf(c[2][r])), __builtin_fabsf(c[2][r + 1]));
        mb = __builtin_fmaxf(__builtin_fmaxf(mb, __builtin_fabsf(c[3][r])), __builtin_fabsf(c[3][r + 1]));
      }
      const unsigned m = wave_max_u32(jok ? max(__float_as_uint(ma), oi < 2 ? __float_as_uint(mb) : 0u) : 0u);
      if ((lane & 15) == 15) lds_max(C_MISC + 8, m);
    }
    lbar(4);
    k2 = min(14 - __builtin_amdgcn_readfirstlane(__builtin_amdgcn_frexp_expf(ldsr<float>(C_MISC + 8))), 120);
    const unsigned da = C_WF + (it >> 3) * FP + ((it >> 2) & 1) * 528 + (it & 3) * 4;
    const bool gat = w >= 1 && it < 176;
    if (jok) {
      const float sc2 = __uint_as_float((127 + k2) << 23);
      const unsigned tw = opaque(C_T + (oj * 53 + 8 * hh) * 32 + oi * 4);
      const unsigned lw = C_WF + (44 + ((oj >= 13 ? oj - 13 : oj) >> 3)) * FP + (oj >= 13 ? 528 : 0) + oi * 16 + ((oj >= 13 ? oj - 13 : oj) & 7) * 2;
      if (hh == 0) {
#pragma unroll
        for (int i = 0; i < 4; ++i) ldsw<u16>(lw + i * 128, (i < 3 || oi < 2) ? (u16)pack_bf16(c[i][14], 0.f) : (u16)0);
      }
#pragma unroll
      for (int i = 0; i < 4; ++i) c[i] *= sc2;
#pragma unroll
      for (int r = 0; r < 12; ++r) {
        ldsw<unsigned>(tw + ((r & 3) + 8 * (r >> 2)) * 64, pack_f16(c[0][r], c[1][r]));
        ldsw<unsigned>(tw + ((r & 3) + 8 * (r >> 2)) * 64 + 32, pack_f16(c[2][r], c[3][r]));
      }
      if (hh == 0) {
#pragma unroll
        for (int r = 12; r < 14; ++r) {
          ldsw<unsigned>(tw + (r + 12) * 64, pack_f16(c[0][r], c[1][r]));
          ldsw<unsigned>(tw + (r + 12) * 64 + 32, pack_f16(c[2][r], c[3][r]));
        }
      }
    }
    if (gat) {
#pragma unroll
      for (int i = 0; i < 13; ++i) ldsw<unsigned>(da + ig * (13 * 16) + i * 16, fw[i]);
      if (ig) {
#pragma unroll
        for (int i = 26; i < 32; ++i) { ldsw<unsigned>(da + i * 16, 0u); ldsw<unsigned>(da + 22 * FP + i * 16, 0u); }
      }
    }
    if (w == 0) { ldsw<u16>(C_WF + 45 * FP + hh * 528 + (lane & 31) * 16 + 10, (u16)0); ldsw<unsigned>(C_WF + 45 * FP + hh * 528 + (lane & 31) * 16 + 12, 0u); }
    lbar(5);
    if (gat) {
      const unsigned base = C_T + ig * 32;
      const unsigned g0 = base + (tb.x & 0xffff) * 32, g1 = base + ((tb.x >> 16) & 0x7fff) * 32, g2 = base + (tb.y & 0xffff) * 32, g3 = base + ((tb.y >> 16) & 0x7fff) * 32;
      f16x8 x0[2], y0[2], x1[2], y1[2];
#pragma unroll
      for (int i = 0; i < 2; ++i) { x0[i] = ldsr<f16x8>(g0 + i * 16); y0[i] = ldsr<f16x8>(g1 + i * 16); x1[i] = ldsr<f16x8>(g2 + i * 16); y1[i] = ldsr<f16x8>(g3 + i * 16); }
      const _Float16 m0 = (int)tb.x < 0 ? 0 : 1, m1 = (int)tb.y < 0 ? 0 : 1;
      const unsigned db = da + 22 * FP + ig * 256;
#pragma unroll
      for (int i = 0; i < 2; ++i) {
        const u32x4 e0 = __builtin_bit_cast(u32x4, __builtin_elementwise_fma(y0[i], f16x8{m0, m0, m0, m0, m0, m0, m0, m0}, x0[i]));
        const u32x4 e1 = __builtin_bit_cast(u32x4, __builtin_elementwise_fma(y1[i], f16x8{m1, m1, m1, m1, m1, m1, m1, m1}, x1[i]));
#pragma unroll
        for (int p = 0; p < 4; ++p) {
          ldsw<unsigned>(db + (4 * i + p) * 16, __builtin_amdgcn_perm(e1[p], e0[p], 0x05040100));
          if (4 * i + p < 2) ldsw<unsigned>(db + (4 * i + p + 8) * 16, __builtin_amdgcn_perm(e1[p], e0[p], 0x07060302));
          else if (!ig) ldsw<unsigned>(db + (4 * i + p + 8) * 16, __builtin_amdgcn_perm(e1[p], e0[p], 0x07060302));
        }
      }
    }
    waitv<0>();
    lbar(6);
    wl = C_WF + (lane & 31) * 16 + hh * 528;
    if (w == 6) {
      const u32x4 v = ldsr<u32x4>(wl + min(cw, 45) * FP);
      __builtin_amdgcn_raw_buffer_store_b128(v, __builtin_amdgcn_make_buffer_rsrc(ws + WS_W, 0, 46 * 1024, 0x00020000), lane * 16, cw * 1024, 16);
      waitv<0>();
      if (lane == 0) __hip_atomic_store((GLB_AS u64*)(ws + WS_WFL) + cw, tag | (u64)(((k1 + 2048) << 12) | (k2 + 2048)), __ATOMIC_RELAXED, __HIP_MEMORY_SCOPE_AGENT);
    }
  }
  if (!off) __builtin_amdgcn_s_sleep(XD_LATE);
  {
    const int lane = lane_id(), hh = lane >> 5, tid = w * 64 + lane; const unsigned xch = 4096 + (lane & 7) * 16; (void)hh; (void)tid; (void)xch;
    f32x16 a1i;
#pragma unroll
    for (int g = 0; g < 8; ++g) { a1i[2 * g] = bf_lo(a1q[g]); a1i[2 * g + 1] = bf_hi(a1q[g]); }
    const unsigned xl = (lane & 31) * 4 + hh * 1664, xh = (lane & 31) * 4 - hh * 1664, hmv = hh ? 0xffff0000u : 0u;
#pragma nounroll
    while (u < ucnt) {
      auto nextu = [&](int v) { return v < 14 || (v < 21 && w < 4) ? v + NCW : (w == 3 && v == 24 ? 25 : ucnt); };
      const int nx = off ? ucnt : nextu(u);
      if (!off) {
        cur = xaddr(u);
        if (nx < ucnt) { xload(rsA, ubase, nx, xaddr(nx), lane, xch); waitv<4>(); } else waitv<0>();
      }
      float res = 0.f;
      asm volatile(CIN_UNIT_ASM
                   : [res] "=v"(res)
                   : [xlo] "v"(cur + xl), [xhi] "v"(cur + xh), [wl] "v"(wl), [a1] "v"(a1i), [hm] "s"(0xffffffff00000000ull), [hmv] "v"(hmv),
                     [psel] "s"(0x05040100), [pselh] "s"(0x07060302), [k1] "s"(16 + k1), [k2] "s"(16 + k2), [off] "s"(off),
                     [rsf] "s"(rsrc4(ws + WS_W, 46 * 1024)), [wm] "s"((w & 3) * FP), [wo] "s"((w & 3) * 1024)
                   : "memory", "scc", "vcc", CIN_UNIT_CLOB);
      asm volatile("s_mov_b32 exec_lo, 0\n\ts_mov_b32 exec_hi, 0x80000000\n\tds_write_b32 %0, %1\n\ts_waitcnt lgkmcnt(0)\n\tds_add_u32 %2, %3\n\ts_mov_b64 exec, -1"
                   ::"v"(off ? G_IDX + 3336 + (w - 4) * 4 : C_RES + u * 4), "v"(res), "v"(off ? off : C_MISC + 20), "v"(1) : "memory");
      u = nx;
    }
  }
  if (j < 6) return;
  if (w == 5 && bid == 48) {
    const int lane = lane_id();
    const GLB_AS u64* gb = (const GLB_AS u64*)(ws + WS_GRAN);
    const GLB_AS u64* gp[4];
#pragma unroll
    for (int i = 0; i < 4; ++i) {
      const int q = i * 64 + lane;
      gp[i] = q < 208 ? gb + (q >> 3) * 2048 + ((q >> 1) & 3) * 256 + (q & 1) * 64 : (const GLB_AS u64*)(ws + WS_WFL) + (q - 208);
    }
    for (int spin = 0; spin <= (1 << 17); ++spin) {
      bool ok = true;
#pragma unroll
      for (int i = 0; i < 4; ++i) ok &= ((__hip_atomic_load(gp[i], __ATOMIC_RELAXED, __HIP_MEMORY_SCOPE_AGENT) ^ tag) >> 24) == 0;
      if (__all(ok)) break;
      __builtin_amdgcn_s_sleep(4);
    }
    asm volatile(".Lxd_end:");
    if (lane == 0) asm volatile("global_store_dwordx2 %0, %1, %2 " XD_OUTF ::"v"(0), "v"(ep + 1), "s"(ws) : "memory");
  }
#endif
}

extern "C" void kernel_launch(void* const* d_in, const int* in_sizes, int n_in, void* d_out, int out_size, void* d_ws, size_t ws_size, hipStream_t stream) {
  (void)in_sizes; (void)n_in; (void)out_size; (void)ws_size;
  (void)hipFuncSetAttribute((const void*)xdeepfm_kernel, hipFuncAttributeMaxDynamicSharedMemorySize, LDS_BYTES);
  xdeepfm_kernel<<<256, 512, LDS_BYTES, stream>>>((const int*)d_in[0], (const u16*)d_in[1], (const u16*)d_in[2], (const u16*)d_in[3], (const u16*)d_in[4],
                                                  (const u16*)d_in[5], (const u16*)d_in[6], (const u16*)d_in[7], (u16*)d_out, (char*)d_ws);
}
```
